```python
import torch

def _softmax(x, axis=-1):
    return torch.softmax(x, dim=axis)
def _gelu(x):
    return torch.nn.functional.gelu(x, approximate="tanh")
def _silu(x):
    return torch.nn.functional.silu(x)
def _sigmoid(x):
    return torch.sigmoid(x)
def _layernorm(x, axis=-1, eps=1e-5):
    m = x.mean(dim=axis, keepdim=True)
    v = x.var(dim=axis, keepdim=True, unbiased=False)
    return (x - m) / torch.sqrt(v + eps)
def _scatter_add(tgt, idx, src, axis=0):
    return tgt.clone().index_add_(axis, idx, src)
def _segment_sum(src, seg, n):
    out = torch.zeros((n,) + src.shape[1:], dtype=src.dtype)
    return out.index_add_(0, seg, src)
def _pad(x, pw):
    flat = tuple(v for pair in reversed(pw) for v in pair)
    return torch.nn.functional.pad(x, flat)

kernel_name = 'recurrent_scan__ssm_conv_s4__dense_activation_gelu__prompt'

def setup_inputs():

    _w = (lambda a: (lambda t: t.to(torch.bfloat16) if t.is_floating_point() else t)(torch.as_tensor(a)))
    def _A_setup():
        B, L, D, N = 1, 2048, 640, 192
        A_re = -(lambda _x: torch.exp(torch.as_tensor(_x)))((lambda *s: torch.randn(*s, dtype=torch.bfloat16) * 3.2)(D, N))
        A_im = torch.pi * (lambda *s: torch.randn(*s, dtype=torch.bfloat16) * 0.2)(D, N)
        B_re = (lambda *s: torch.randn(*s, dtype=torch.bfloat16) * 0.2)(D, N)
        B_im = (lambda *s: torch.randn(*s, dtype=torch.bfloat16) * 0.2)(D, N)
        C_re = (lambda *s: torch.randn(*s, dtype=torch.bfloat16) * 0.2)(D, N)
        C_im = (lambda *s: torch.randn(*s, dtype=torch.bfloat16) * 0.2)(D, N)
        log_dt = (lambda *s: torch.randn(*s, dtype=torch.bfloat16) * 0.2)(D) * 0.5 - 4.0
        Dskip = (lambda *s: torch.randn(*s, dtype=torch.bfloat16) * 0.2)(D)
        return {
            "u": _w((lambda *s: torch.randn(*s, dtype=torch.bfloat16) * 0.05)(B, L, D)),
            "A_re": _w(A_re),
            "A_im": _w(A_im),
            "B_re": _w(B_re),
            "B_im": _w(B_im),
            "C_re": _w(C_re),
            "C_im": _w(C_im),
            "log_dt": _w(log_dt),
            "Dskip": _w(Dskip),
        }
    _A_in = _A_setup()

    def _B_setup():
        B, S, D, F = 1, 2048, 640, 1536
        return {
            "x": _w((lambda *s: torch.randn(*s, dtype=torch.bfloat16) * 0.05)(B, S, D)),
            "w1": _w((lambda *s: torch.randn(*s, dtype=torch.bfloat16) * 0.8)(D, F) * (D ** -0.5)),
            "b1": _w((lambda *s: torch.randn(*s, dtype=torch.bfloat16) * 0.1)(F) * 0.01),
            "w2": _w((lambda *s: torch.randn(*s, dtype=torch.bfloat16) * 0.1)(F, D) * (F ** -0.5)),
            "b2": _w((lambda *s: torch.randn(*s, dtype=torch.bfloat16) * 0.1)(D) * 0.01),
        }
    _B_in = _B_setup()

    return {
        "A_u": _A_in["u"],
        "A_A_re": _A_in["A_re"],
        "A_A_im": _A_in["A_im"],
        "A_B_re": _A_in["B_re"],
        "A_B_im": _A_in["B_im"],
        "A_C_re": _A_in["C_re"],
        "A_C_im": _A_in["C_im"],
        "A_log_dt": _A_in["log_dt"],
        "A_Dskip": _A_in["Dskip"],
        "B_x": _B_in["x"],
        "B_w1": _B_in["w1"],
        "B_b1": _B_in["b1"],
        "B_w2": _B_in["w2"],
        "B_b2": _B_in["b2"],
    }

def reference(A_u, A_A_re, A_A_im, A_B_re, A_B_im, A_C_re, A_C_im, A_log_dt, A_Dskip, B_x, B_w1, B_b1, B_w2, B_b2):
    def _A_ref(u, A_re, A_im, B_re, B_im, C_re, C_im, log_dt, Dskip):
        Bsz, L, D = u.shape
        dt = torch.exp(log_dt)[:, None]
        A = A_re + 1j * A_im
        Bc = B_re + 1j * B_im
        Cc = C_re + 1j * C_im
        logdA = dt * A
        dB = dt * Bc
        l = (lambda x, dt: x.to(dt))(torch.arange(L), A_re.dtype)
        P = torch.exp(logdA[:, :, None] * l[None, None, :])
        K = torch.einsum("dn,dnl,dn->dl", Cc, P, dB).real
        L2 = 2 * L
        Kp = _pad(K, ((0, 0), (0, L)))
        up = (lambda x, *d: x.permute(*d))(u, 0, 2, 1)
        up = _pad(up, ((0, 0), (0, 0), (0, L)))
        Kf = (lambda x, axes=(-2,-1): torch.fft.rfft2(x.float(), dim=axes))(Kp[:, None, :], axes=(-2, -1))
        Uf = (lambda x, axes=(-2,-1): torch.fft.rfft2(x.float(), dim=axes))(up[:, :, None, :], axes=(-2, -1))
        Yf = Uf * Kf[None, :, :, :]
        y = (lambda x, s, axes=(-2,-1): torch.fft.irfft2(x, s=s, dim=axes).to(torch.bfloat16))(Yf, (1, L2), axes=(-2, -1))[:, :, 0, :L]
        y = (lambda x, *d: x.permute(*d))(y, 0, 2, 1) + u * Dskip[None, None, :]
        return y

    def _B_ref(x, w1, b1, w2, b2):
        h = torch.einsum("bsd,df->bsf", x, w1) + b1
        h = _gelu(h)
        return torch.einsum("bsf,fd->bsd", h, w2) + b2

    x = A_u
    xn = _layernorm(x, axis=-1)
    s = _A_ref(xn, A_A_re, A_A_im, A_B_re, A_B_im, A_C_re, A_C_im, A_log_dt, A_Dskip)
    g = _silu(_B_ref(xn, B_w1, B_b1, B_w2, B_b2))
    return x + s * g
```

```cpp
#include <hip/hip_runtime.h>
#include <hip/hip_bf16.h>
#include <stdint.h>

typedef unsigned short u16;
typedef __attribute__((ext_vector_type(8))) short bf16x8;
typedef __attribute__((ext_vector_type(4))) float f32x4;

constexpr int L = 2048, D = 640, N = 192, F = 1536;

__device__ __forceinline__ float bf2f(u16 v) { return __uint_as_float(((uint32_t)v) << 16); }
__device__ __forceinline__ u16 f2bf(float f) { __hip_bfloat16 b = __float2bfloat16(f); return *reinterpret_cast<u16*>(&b); }
__device__ __forceinline__ float rbf(float f) { return bf2f(f2bf(f)); }

__global__ void ln_k(const u16* x, u16* xn, u16* xnT) {
  int row = blockIdx.x; int t = threadIdx.x;
  float v[10]; float s = 0;
  for (int i = 0; i < 10; ++i) { v[i] = bf2f(x[row * D + t + 64 * i]); s += v[i]; }
  for (int o = 32; o; o >>= 1) s += __shfl_xor(s, o);
  float m = s / D; float q = 0;
  for (int i = 0; i < 10; ++i) { float d = v[i] - m; q += d * d; }
  for (int o = 32; o; o >>= 1) q += __shfl_xor(q, o);
  float var = rbf(q / D); float mb = rbf(m);
  float den = rbf(sqrtf(rbf(var + 1e-5f)));
  for (int i = 0; i < 10; ++i) {
    u16 r = f2bf(rbf(v[i] - mb) / den);
    xn[row * D + t + 64 * i] = r; xnT[(t + 64 * i) * L + row] = r;
  }
}

__global__ void kgen_k(const u16* Are, const u16* Aim, const u16* Bre, const u16* Bim, const u16* Cre, const u16* Cim,
                       const u16* logdt, float* K) {
  int d = blockIdx.x; int t = threadIdx.x;
  __shared__ float a[N], b[N], wr[N], wi[N];
  float dt = rbf(expf(bf2f(logdt[d])));
  if (t < N) {
    a[t] = dt * bf2f(Are[d * N + t]); b[t] = dt * bf2f(Aim[d * N + t]);
    float br = dt * bf2f(Bre[d * N + t]), bi = dt * bf2f(Bim[d * N + t]);
    float cr = bf2f(Cre[d * N + t]), ci = bf2f(Cim[d * N + t]);
    wr[t] = cr * br - ci * bi; wi[t] = cr * bi + ci * br;
  }
  __syncthreads();
  for (int l = t; l < L; l += 256) {
    float lf = rbf((float)l); float s = 0;
    for (int n = 0; n < N; ++n) {
      float e = __expf(a[n] * lf); float sn, cs; sincosf(b[n] * lf, &sn, &cs);
      s += e * (wr[n] * cs - wi[n] * sn);
    }
    K[d * L + l] = s;
  }
}

__global__ void conv_k(const u16* xnT, const float* K, u16* y) {
  int d = blockIdx.x; int t = threadIdx.x;
  __shared__ float su[L], sk[L];
  for (int i = t; i < L; i += 256) { su[i] = bf2f(xnT[d * L + i]); sk[i] = K[d * L + i]; }
  __syncthreads();
  for (int l = t; l < L; l += 256) {
    float s = 0;
    for (int j = 0; j <= l; ++j) s += sk[l - j] * su[j];
    y[l * D + d] = f2bf(s);
  }
}

__device__ __forceinline__ float gelu_t(float x) {
  float u = 0.7978845608028654f * (x + 0.044715f * x * x * x);
  return 0.5f * x * (1.f + tanhf(u));
}

template <int MODE>
__global__ void gemm_k(const u16* A, const u16* B, const u16* bias, u16* C, int Kd, int Nn,
                       const u16* x, const u16* xn, const u16* y, const u16* Dskip) {
  __shared__ u16 sA[16][32], sB[32][16];
  int bx = blockIdx.x * 16, by = blockIdx.y * 16, lane = threadIdx.x, row = lane & 15, quad = lane >> 4;
  f32x4 acc = {0, 0, 0, 0};
  for (int k0 = 0; k0 < Kd; k0 += 32) {
    for (int e = lane; e < 512; e += 64) {
      int r = e / 32, c = e % 32;
      sA[r][c] = A[(by + r) * Kd + k0 + c];
      sB[c][r] = B[(k0 + c) * Nn + bx + r];
    }
    __syncthreads();
    bf16x8 a, b;
    for (int j = 0; j < 8; ++j) { a[j] = sA[row][quad * 8 + j]; b[j] = sB[quad * 8 + j][row]; }
    acc = __builtin_amdgcn_mfma_f32_16x16x32_bf16(a, b, acc, 0, 0, 0);
    __syncthreads();
  }
  for (int j = 0; j < 4; ++j) {
    int r = by + quad * 4 + j, c = bx + row;
    float h = rbf(rbf(acc[j]) + bf2f(bias[c]));
    if (MODE == 0) C[r * Nn + c] = f2bf(gelu_t(h));
    else {
      float g = rbf(h / (1.f + __expf(-h)));
      float s = rbf(bf2f(y[r * D + c]) + rbf(bf2f(xn[r * D + c]) * bf2f(Dskip[c])));
      C[r * Nn + c] = f2bf(bf2f(x[r * D + c]) + rbf(s * g));
    }
  }
}

extern "C" void kernel_launch(void* const* d_in, const int* in_sizes, int n_in, void* d_out, int out_size, void* d_ws,
                              size_t ws_size, hipStream_t stream) {
  const u16* x = (const u16*)d_in[0];
  char* ws = (char*)d_ws;
  u16* xn = (u16*)ws; u16* xnT = (u16*)(ws + (4 << 20)); float* K = (float*)(ws + (8 << 20));
  u16* y = (u16*)(ws + (16 << 20)); u16* h = (u16*)(ws + (20 << 20));
  ln_k<<<L, 64, 0, stream>>>(x, xn, xnT);
  kgen_k<<<D, 256, 0, stream>>>((const u16*)d_in[1], (const u16*)d_in[2], (const u16*)d_in[3], (const u16*)d_in[4],
                                (const u16*)d_in[5], (const u16*)d_in[6], (const u16*)d_in[7], K);
  conv_k<<<D, 256, 0, stream>>>(xnT, K, y);
  gemm_k<0><<<dim3(F / 16, L / 16), 64, 0, stream>>>(xn, (const u16*)d_in[10], (const u16*)d_in[11], h, D, F, 0, 0, 0, 0);
  gemm_k<1><<<dim3(D / 16, L / 16), 64, 0, stream>>>(h, (const u16*)d_in[12], (const u16*)d_in[13], (u16*)d_out, F, D, x, xn, y,
                                                     (const u16*)d_in[8]);
}
```

```cpp
#include <hip/hip_runtime.h>
#include <stdint.h>

typedef unsigned u32;
typedef unsigned short u16;
typedef unsigned long long u64;
typedef __attribute__((ext_vector_type(8))) __bf16 bf16x8;
typedef __attribute__((ext_vector_type(2))) __bf16 bf16x2;
typedef __attribute__((ext_vector_type(4))) short s16x4;
typedef __attribute__((ext_vector_type(2))) float f32x2;
typedef __attribute__((ext_vector_type(4))) float f32x4;
typedef __attribute__((ext_vector_type(16))) float f32x16;
typedef __attribute__((ext_vector_type(2))) u32 u32x2;
typedef __attribute__((ext_vector_type(4))) u32 u32x4;
typedef __attribute__((ext_vector_type(2), aligned(4))) u32 u32x2a4;
#define LAS __attribute__((address_space(3)))
#define GAS __attribute__((address_space(1)))
typedef __amdgpu_buffer_rsrc_t rsrc_t;

constexpr int D = 640, NM = 192;
constexpr u32 WS_LNF = 4096, WS_W2F = 8192, WS_SF = 12288, WS_HF = 16384, WS_ST = 65536;
constexpr u32 WS_XN = 1u << 20, WS_SP = 4u << 20, WS_W2P = 8u << 20, WS_HP = 12u << 20, WS_STAMP = 20u << 20, WS_SPAN = 24u << 20;
constexpr u32 TAG_ADD = 0x9E3779B9u;
#define SPINS 400000
constexpr int SPIN_MAX = SPINS;
constexpr int LDS_BYTES = 81920;

#define MAP_M8 0
#define PF_MODE 2

#define STAMP(k)
#define STAMPS_DECL
#define STAMPR(k)
#define STAMPW(k)
#define STAMPH(k)

__device__ __forceinline__ float bflo(u32 v) { return __uint_as_float(v << 16); }
__device__ __forceinline__ float bfhi(u32 v) { return __uint_as_float(v & 0xffff0000u); }
__device__ __forceinline__ u32 pkbf(float a, float b) {
  f32x2 t = {a, b};
  return __builtin_bit_cast(u32, __builtin_convertvector(t, bf16x2));
}
__device__ __forceinline__ float rbf(float a) { return bflo(pkbf(a, 0.f)); }
__device__ __forceinline__ f32x2 cmul(f32x2 a, f32x2 b) {
  f32x2 t, d;
  asm("v_pk_mul_f32 %0, %1, %2 op_sel:[0,0] op_sel_hi:[0,1]" : "=v"(t) : "v"(a), "v"(b));
  asm("v_pk_fma_f32 %0, %1, %2, %3 op_sel:[1,1,0] op_sel_hi:[1,0,1] neg_lo:[1,0,0]" : "=v"(d) : "v"(a), "v"(b), "v"(t));
  return d;
}
__device__ __forceinline__ f32x4 mfma16(u32x4 a, u32x4 b, f32x4 c) {
  return __builtin_amdgcn_mfma_f32_16x16x32_bf16(__builtin_bit_cast(bf16x8, a), __builtin_bit_cast(bf16x8, b), c, 0, 0, 0);
}
__device__ __forceinline__ f32x16 mfma32(u32x4 a, u32x4 b, f32x16 c) {
  return __builtin_amdgcn_mfma_f32_32x32x16_bf16(__builtin_bit_cast(bf16x8, a), __builtin_bit_cast(bf16x8, b), c, 0, 0, 0);
}
__device__ __forceinline__ u32x2 trrd(LAS char* p) {
  return __builtin_bit_cast(u32x2, __builtin_amdgcn_ds_read_tr16_b64_v4i16((LAS s16x4*)p));
}
__device__ __forceinline__ u32x4 trrd2(LAS char* p0, LAS char* p1) {
  u32x2 a = trrd(p0), b = trrd(p1);
  return (u32x4){a.x, a.y, b.x, b.y};
}
__device__ __forceinline__ rsrc_t mkrs(const void* p, u32 bytes) {
  return __builtin_amdgcn_make_buffer_rsrc((void*)p, (short)0, bytes, 0x00020000);
}
#define WAITVM(n) asm volatile("s_waitcnt vmcnt(" #n ")" ::: "memory")
#define BAR() asm volatile("s_barrier" ::: "memory")
#define WAITLGKM0() asm volatile("s_waitcnt lgkmcnt(0)" ::: "memory")
#define SCHED0() __builtin_amdgcn_sched_barrier(0)

#define DMA16(m0v, voff, rs, soff) \
  asm volatile("s_mov_b32 m0, %0\n\ts_nop 0\n\tbuffer_load_dwordx4 %1, %2, %3 offen lds" ::"s"(m0v), "v"(voff), "s"(rs), "s"(soff) : "memory")
#define LD128SC1(dst, voff, rs, soff, OFF) \
  asm volatile("buffer_load_dwordx4 %0, %1, %2, %3 offen offset:" #OFF " sc1" : "=v"(dst) : "v"(voff), "s"(rs), "s"(soff) : "memory")
#define LD64SC1(dst, voff, rs, soff, OFF) \
  asm volatile("buffer_load_dwordx2 %0, %1, %2, %3 offen offset:" #OFF " sc1" : "=v"(dst) : "v"(voff), "s"(rs), "s"(soff) : "memory")
#define LD64(dst, voff, rs, soff, OFF) \
  asm volatile("buffer_load_dwordx2 %0, %1, %2, %3 offen offset:" #OFF : "=v"(dst) : "v"(voff), "s"(rs), "s"(soff) : "memory")
#define LD32SC1(dst, voff, rs, soff) \
  asm volatile("buffer_load_dword %0, %1, %2, %3 offen sc1" : "=v"(dst) : "v"(voff), "s"(rs), "s"(soff) : "memory")
#define ST128SC1(src, voff, rs, soff, OFF) \
  asm volatile("buffer_store_dwordx4 %0, %1, %2, %3 offen offset:" #OFF " sc1\n\ts_nop 1" ::"v"(src), "v"(voff), "s"(rs), "s"(soff) : "memory")
#define PIN1(a) asm volatile("" : "+v"(a))

__device__ __forceinline__ void poll_flag(u32 voff, rsrc_t rs, u32 tag) {
  _Pragma("unroll 1") for (int sp = 0; sp < SPIN_MAX; ++sp) {
    u32 v;
    asm volatile("buffer_load_dword %0, %1, %2, 0 offen sc1\n\ts_waitcnt vmcnt(0)" : "=v"(v) : "v"(voff), "s"(rs) : "memory");
    if (__all(v == tag)) break;
    __builtin_amdgcn_s_sleep(1);
  }
}

template <int CTRL>
__device__ __forceinline__ float dpp_add(float v) {
  return v + __int_as_float(__builtin_amdgcn_update_dpp(0, __float_as_int(v), CTRL, 0xf, 0xf, true));
}
__device__ __forceinline__ float red32(float v) {
  v = dpp_add<0xB1>(v);
  v = dpp_add<0x4E>(v);
  v = dpp_add<0x141>(v);
  v = dpp_add<0x140>(v);
  auto s = __builtin_amdgcn_permlane16_swap(__float_as_uint(v), __float_as_uint(v), false, false);
  return __uint_as_float(s[0]) + __uint_as_float(s[1]);
}
__device__ __forceinline__ u32 shl1(u32 v) { return (u32)__builtin_amdgcn_update_dpp(0, (int)v, 0x130, 0xf, 0xf, true); }
__device__ __forceinline__ u32 lohi(u32 lo, u32 hi) { return (lo & 0xffffu) | (hi & 0xffff0000u); }

#define WAITLGKM0B() __builtin_amdgcn_s_waitcnt(0xC07F)
#define DMA16S(m0v, voff, rs, soff, OFF) \
  asm volatile("s_mov_b32 m0, %0\n\ts_nop 0\n\tbuffer_load_dwordx4 %1, %2, %3 offen offset:" #OFF " sc1 lds" ::"s"(m0v), "v"(voff), "s"(rs), "s"(soff) : "memory")
#define WAITVM_PRO() WAITVM(17)
#define WAITVM_PRO0() WAITVM(20)
typedef const __attribute__((address_space(4))) u32* cptr32;

__device__ __forceinline__ void gemm_role(const u16* x, const u16* w1, const u16* b1, const u16* b2, u16* out, char* ws, LAS char* L) {
  const int tid = threadIdx.x, lane = tid & 63;
  const int w = __builtin_amdgcn_readfirstlane(tid >> 6);
  const int b = blockIdx.x, xcd = b & 7, r = b >> 3;
  const int ti = (xcd >> 1) * 8 + (r >> 2), tj = (xcd & 1) * 4 + (r & 3);
  STAMPS_DECL
  STAMPR(0);
  const rsrc_t rx = mkrs(x, 2048 * 640 * 2), rw1 = mkrs(w1, 640 * 1536 * 2), rws = mkrs(ws, WS_SPAN);
  const rsrc_t rb1 = mkrs(b1, 1536 * 2), rb2 = mkrs(b2, 640 * 2), rout = mkrs(out, 2048 * 640 * 2);
  const u32 lbase = (u32)(uintptr_t)L;
  const int wm = w & 1, wn = w >> 1;
  const int g = lane >> 4, m16 = lane & 15, q4 = (lane >> 2) & 3, p4 = lane & 3;
  u32 tag = *(cptr32)(uintptr_t)ws + TAG_ADD;
  u32 vx[2], vw;
  {
    const int cp = lane & 7;
#pragma unroll
    for (int h = 0; h < 2; ++h) {
      const int row = 16 * w + 8 * h + (lane >> 3);
      vx[h] = (64 * ti + row) * 1280 + ((cp ^ ((row >> 1) & 7)) << 4);
    }
    const int row = 8 * w + (lane >> 3);
    const int hh = ((row >> 1) & 1) | (((row >> 3) & 1) << 1);
    vw = row * 3072 + tj * 384 + ((cp ^ (hh << 1)) << 4);
  }
  const u32 m0x = lbase + w * 2048, m0w = lbase + 16384 + w * 1024, m0p = lbase + 16384 + w * 3072;
  DMA16(m0x, vx[0], rx, 0);
  DMA16(m0x + 1024, vx[1], rx, 0);
#pragma unroll
  for (int p = 0; p < 3; ++p) DMA16(m0w + p * 4096, vw, rw1, p * 128);
#pragma unroll
  for (int p = 0; p < 3; ++p) DMA16(m0w + 12288 + p * 4096, vw, rw1, 98304 + p * 128);
  DMA16(m0x + 8192, vx[0], rx, 128);
  DMA16(m0x + 8192 + 1024, vx[1], rx, 128);
#pragma unroll
  for (int p = 0; p < 3; ++p) DMA16(m0w + 2 * 12288 + p * 4096, vw, rw1, 2 * 98304 + p * 128);
#pragma unroll
  for (int p = 0; p < 3; ++p) DMA16(m0w + 3 * 12288 + p * 4096, vw, rw1, 3 * 98304 + p * 128);
#pragma unroll
  for (int u = 0; u < 3; ++u) {
    u32 lam = ((u * 32 + r) * 4 + w) * 32 + (lane >> 1);
    if (lam >= 10240) lam -= 4096;
    const u32 st = 2 + lam / 1280, wi = lam % 1280;
    const u32 y = wi - 512;
    const bool isx = wi < 512;
    const u32 off = isx ? ((xcd >> 1) * 512 + wi) * 1280 + st * 128 : (st * 64 + y / 12) * 3072 + (xcd & 1) * 1536 + (y % 12) * 128;
    const char* ptr = (isx ? (const char*)x : (const char*)w1) + off + (lane & 1) * 64;
    asm volatile("s_mov_b32 m0, %0\n\ts_nop 0\n\tglobal_load_lds_dword %1, off" ::"s"(lbase + 80896 + w * 256), "v"(ptr) : "memory");
  }
  u32x2 b1r[6];
  {
    const u32 vb = (192 * tj + 96 * wn + 4 * g) * 2;
#pragma unroll
    for (int f = 0; f < 6; ++f) LD64(b1r[f], vb, rb1, 32 * (wm ? 5 - f : f), 0);
  }
  LAS char* aw[6];
  LAS char* ax[2];
  {
    const int hl = ((q4 >> 1) & 1) | ((g & 1) << 1);
#pragma unroll
    for (int f = 0; f < 6; ++f) {
      const int nfg = 6 * wn + (wm ? 5 - f : f);
      aw[f] = L + 16384 + (nfg >> 2) * 4096 + (8 * g + q4) * 128 + (((nfg & 3) ^ hl) << 5) + 8 * p4;
    }
    const int row = 32 * wm + m16;
#pragma unroll
    for (int ks = 0; ks < 2; ++ks) ax[ks] = L + row * 128 + (((4 * ks + g) ^ ((row >> 1) & 7)) << 4);
  }
  const f32x4 zero4 = {0.f, 0.f, 0.f, 0.f};
  f32x4 acc[6][2], cs[6];
#pragma unroll
  for (int f = 0; f < 6; ++f) { acc[f][0] = zero4; acc[f][1] = zero4; cs[f] = zero4; }
  const u32x4 ones = {0x3F803F80u, 0x3F803F80u, 0x3F803F80u, 0x3F803F80u};
  u32x2 stt[2];
  u32 w2fv;
  const u32 vst = WS_ST + (64 * ti + 32 * wm + m16) * 8;
  const u32 vw2f = WS_W2F + 4 * (2 * (12 * w + (lane < 12 ? lane : 11)) + (tj >> 2));
  const u32 vl16 = lane * 16;
  const u32 w2base = WS_W2P + (tj * 48 + 12 * w) * 5120;
  asm volatile("" : "+s"(tag));
  u32x4 A0[6], A1[6], X0[2][2], X1[2][2];
  WAITVM_PRO0();
  BAR();
  STAMPR(1);
#pragma unroll
  for (int ks = 0; ks < 2; ++ks) { X0[ks][0] = *(LAS u32x4*)(ax[ks]); X0[ks][1] = *(LAS u32x4*)(ax[ks] + 2048); }
#pragma unroll
  for (int f = 0; f < 6; ++f) A0[f] = trrd2(aw[f], aw[f] + 512);
  WAITVM_PRO();
  WAITLGKM0B();
  BAR();
  STAMPH(12);
  SCHED0();
  A1[0] = trrd2(aw[0] + 12288, aw[0] + 12800);
  A1[1] = trrd2(aw[1] + 12288, aw[1] + 12800);
  acc[0][0] = mfma16(A0[0], X0[0][0], acc[0][0]);
  acc[0][1] = mfma16(A0[0], X0[0][1], acc[0][1]);
  cs[0] = mfma16(A0[0], ones, cs[0]);
  SCHED0();
  A1[2] = trrd2(aw[2] + 12288, aw[2] + 12800);
  A1[3] = trrd2(aw[3] + 12288, aw[3] + 12800);
  acc[1][0] = mfma16(A0[1], X0[0][0], acc[1][0]);
  acc[1][1] = mfma16(A0[1], X0[0][1], acc[1][1]);
  cs[1] = mfma16(A0[1], ones, cs[1]);
  DMA16(m0x + 0, vx[0], rx, 256);
  SCHED0();
  A1[4] = trrd2(aw[4] + 12288, aw[4] + 12800);
  A1[5] = trrd2(aw[5] + 12288, aw[5] + 12800);
  acc[2][0] = mfma16(A0[2], X0[0][0], acc[2][0]);
  acc[2][1] = mfma16(A0[2], X0[0][1], acc[2][1]);
  cs[2] = mfma16(A0[2], ones, cs[2]);
  DMA16(m0x + 1024, vx[1], rx, 256);
  SCHED0();
  acc[3][0] = mfma16(A0[3], X0[0][0], acc[3][0]);
  acc[3][1] = mfma16(A0[3], X0[0][1], acc[3][1]);
  DMA16(m0w + 0, vw, rw1, 393216);
  SCHED0();
  acc[4][0] = mfma16(A0[4], X0[0][0], acc[4][0]);
  acc[4][1] = mfma16(A0[4], X0[0][1], acc[4][1]);
  DMA16(m0w + 4096, vw, rw1, 393344);
  SCHED0();
  acc[5][0] = mfma16(A0[5], X0[0][0], acc[5][0]);
  acc[5][1] = mfma16(A0[5], X0[0][1], acc[5][1]);
  DMA16(m0w + 8192, vw, rw1, 393472);
  SCHED0();
  WAITVM_PRO();
  WAITLGKM0B();
  BAR();
  SCHED0();
  X1[0][0] = *(LAS u32x4*)(ax[0] + 8192);
  X1[0][1] = *(LAS u32x4*)(ax[0] + 10240);
  X1[1][0] = *(LAS u32x4*)(ax[1] + 8192);
  acc[0][0] = mfma16(A1[0], X0[1][0], acc[0][0]);
  acc[0][1] = mfma16(A1[0], X0[1][1], acc[0][1]);
  cs[0] = mfma16(A1[0], ones, cs[0]);
  SCHED0();
  X1[1][1] = *(LAS u32x4*)(ax[1] + 10240);
  A0[0] = trrd2(aw[0] + 24576, aw[0] + 25088);
  A0[1] = trrd2(aw[1] + 24576, aw[1] + 25088);
  acc[1][0] = mfma16(A1[1], X0[1][0], acc[1][0]);
  acc[1][1] = mfma16(A1[1], X0[1][1], acc[1][1]);
  cs[1] = mfma16(A1[1], ones, cs[1]);
  DMA16(m0w + 12288, vw, rw1, 491520);
  SCHED0();
  A0[2] = trrd2(aw[2] + 24576, aw[2] + 25088);
  A0[3] = trrd2(aw[3] + 24576, aw[3] + 25088);
  A0[4] = trrd2(aw[4] + 24576, aw[4] + 25088);
  acc[2][0] = mfma16(A1[2], X0[1][0], acc[2][0]);
  acc[2][1] = mfma16(A1[2], X0[1][1], acc[2][1]);
  cs[2] = mfma16(A1[2], ones, cs[2]);
  SCHED0();
  A0[5] = trrd2(aw[5] + 24576, aw[5] + 25088);
  acc[3][0] = mfma16(A1[3], X0[1][0], acc[3][0]);
  acc[3][1] = mfma16(A1[3], X0[1][1], acc[3][1]);
  DMA16(m0w + 16384, vw, rw1, 491648);
  SCHED0();
  acc[4][0] = mfma16(A1[4], X0[1][0], acc[4][0]);
  acc[4][1] = mfma16(A1[4], X0[1][1], acc[4][1]);
  SCHED0();
  acc[5][0] = mfma16(A1[5], X0[1][0], acc[5][0]);
  acc[5][1] = mfma16(A1[5], X0[1][1], acc[5][1]);
  DMA16(m0w + 20480, vw, rw1, 491776);
  SCHED0();
  WAITVM_PRO();
  WAITLGKM0B();
  BAR();
  STAMPH(13);
  SCHED0();
  A1[0] = trrd2(aw[0] + 36864, aw[0] + 37376);
  A1[1] = trrd2(aw[1] + 36864, aw[1] + 37376);
  acc[0][0] = mfma16(A0[0], X1[0][0], acc[0][0]);
  acc[0][1] = mfma16(A0[0], X1[0][1], acc[0][1]);
  cs[0] = mfma16(A0[0], ones, cs[0]);
  SCHED0();
  A1[2] = trrd2(aw[2] + 36864, aw[2] + 37376);
  A1[3] = trrd2(aw[3] + 36864, aw[3] + 37376);
  acc[1][0] = mfma16(A0[1], X1[0][0], acc[1][0]);
  acc[1][1] = mfma16(A0[1], X1[0][1], acc[1][1]);
  cs[1] = mfma16(A0[1], ones, cs[1]);
  DMA16(m0x + 8192, vx[0], rx, 384);
  SCHED0();
  A1[4] = trrd2(aw[4] + 36864, aw[4] + 37376);
  A1[5] = trrd2(aw[5] + 36864, aw[5] + 37376);
  acc[2][0] = mfma16(A0[2], X1[0][0], acc[2][0]);
  acc[2][1] = mfma16(A0[2], X1[0][1], acc[2][1]);
  cs[2] = mfma16(A0[2], ones, cs[2]);
  DMA16(m0x + 9216, vx[1], rx, 384);
  SCHED0();
  acc[3][0] = mfma16(A0[3], X1[0][0], acc[3][0]);
  acc[3][1] = mfma16(A0[3], X1[0][1], acc[3][1]);
  DMA16(m0w + 24576, vw, rw1, 589824);
  SCHED0();
  acc[4][0] = mfma16(A0[4], X1[0][0], acc[4][0]);
  acc[4][1] = mfma16(A0[4], X1[0][1], acc[4][1]);
  DMA16(m0w + 28672, vw, rw1, 589952);
  SCHED0();
  acc[5][0] = mfma16(A0[5], X1[0][0], acc[5][0]);
  acc[5][1] = mfma16(A0[5], X1[0][1], acc[5][1]);
  DMA16(m0w + 32768, vw, rw1, 590080);
  SCHED0();
  WAITVM(8);
  WAITLGKM0B();
  BAR();
  SCHED0();
  X0[0][0] = *(LAS u32x4*)(ax[0] + 0);
  X0[0][1] = *(LAS u32x4*)(ax[0] + 2048);
  X0[1][0] = *(LAS u32x4*)(ax[1] + 0);
  acc[0][0] = mfma16(A1[0], X1[1][0], acc[0][0]);
  acc[0][1] = mfma16(A1[0], X1[1][1], acc[0][1]);
  cs[0] = mfma16(A1[0], ones, cs[0]);
  SCHED0();
  X0[1][1] = *(LAS u32x4*)(ax[1] + 2048);
  A0[0] = trrd2(aw[0] + 0, aw[0] + 512);
  A0[1] = trrd2(aw[1] + 0, aw[1] + 512);
  acc[1][0] = mfma16(A1[1], X1[1][0], acc[1][0]);
  acc[1][1] = mfma16(A1[1], X1[1][1], acc[1][1]);
  cs[1] = mfma16(A1[1], ones, cs[1]);
  DMA16(m0w + 36864, vw, rw1, 688128);
  SCHED0();
  A0[2] = trrd2(aw[2] + 0, aw[2] + 512);
  A0[3] = trrd2(aw[3] + 0, aw[3] + 512);
  A0[4] = trrd2(aw[4] + 0, aw[4] + 512);
  acc[2][0] = mfma16(A1[2], X1[1][0], acc[2][0]);
  acc[2][1] = mfma16(A1[2], X1[1][1], acc[2][1]);
  cs[2] = mfma16(A1[2], ones, cs[2]);
  SCHED0();
  A0[5] = trrd2(aw[5] + 0, aw[5] + 512);
  acc[3][0] = mfma16(A1[3], X1[1][0], acc[3][0]);
  acc[3][1] = mfma16(A1[3], X1[1][1], acc[3][1]);
  DMA16(m0w + 40960, vw, rw1, 688256);
  SCHED0();
  acc[4][0] = mfma16(A1[4], X1[1][0], acc[4][0]);
  acc[4][1] = mfma16(A1[4], X1[1][1], acc[4][1]);
  SCHED0();
  acc[5][0] = mfma16(A1[5], X1[1][0], acc[5][0]);
  acc[5][1] = mfma16(A1[5], X1[1][1], acc[5][1]);
  DMA16(m0w + 45056, vw, rw1, 688384);
  SCHED0();
  WAITVM(8);
  WAITLGKM0B();
  BAR();
  STAMPH(14);
  SCHED0();
  A1[0] = trrd2(aw[0] + 12288, aw[0] + 12800);
  A1[1] = trrd2(aw[1] + 12288, aw[1] + 12800);
  acc[0][0] = mfma16(A0[0], X0[0][0], acc[0][0]);
  acc[0][1] = mfma16(A0[0], X0[0][1], acc[0][1]);
  cs[0] = mfma16(A0[0], ones, cs[0]);
  SCHED0();
  A1[2] = trrd2(aw[2] + 12288, aw[2] + 12800);
  A1[3] = trrd2(aw[3] + 12288, aw[3] + 12800);
  acc[1][0] = mfma16(A0[1], X0[0][0], acc[1][0]);
  acc[1][1] = mfma16(A0[1], X0[0][1], acc[1][1]);
  cs[1] = mfma16(A0[1], ones, cs[1]);
  DMA16(m0x + 0, vx[0], rx, 512);
  SCHED0();
  A1[4] = trrd2(aw[4] + 12288, aw[4] + 12800);
  A1[5] = trrd2(aw[5] + 12288, aw[5] + 12800);
  acc[2][0] = mfma16(A0[2], X0[0][0], acc[2][0]);
  acc[2][1] = mfma16(A0[2], X0[0][1], acc[2][1]);
  cs[2] = mfma16(A0[2], ones, cs[2]);
  DMA16(m0x + 1024, vx[1], rx, 512);
  SCHED0();
  acc[3][0] = mfma16(A0[3], X0[0][0], acc[3][0]);
  acc[3][1] = mfma16(A0[3], X0[0][1], acc[3][1]);
  DMA16(m0w + 0, vw, rw1, 786432);
  SCHED0();
  acc[4][0] = mfma16(A0[4], X0[0][0], acc[4][0]);
  acc[4][1] = mfma16(A0[4], X0[0][1], acc[4][1]);
  DMA16(m0w + 4096, vw, rw1, 786560);
  SCHED0();
  acc[5][0] = mfma16(A0[5], X0[0][0], acc[5][0]);
  acc[5][1] = mfma16(A0[5], X0[0][1], acc[5][1]);
  DMA16(m0w + 8192, vw, rw1, 786688);
  SCHED0();
  WAITVM(8);
  WAITLGKM0B();
  BAR();
  SCHED0();
  X1[0][0] = *(LAS u32x4*)(ax[0] + 8192);
  X1[0][1] = *(LAS u32x4*)(ax[0] + 10240);
  X1[1][0] = *(LAS u32x4*)(ax[1] + 8192);
  acc[0][0] = mfma16(A1[0], X0[1][0], acc[0][0]);
  acc[0][1] = mfma16(A1[0], X0[1][1], acc[0][1]);
  cs[0] = mfma16(A1[0], ones, cs[0]);
  SCHED0();
  X1[1][1] = *(LAS u32x4*)(ax[1] + 10240);
  A0[0] = trrd2(aw[0] + 24576, aw[0] + 25088);
  A0[1] = trrd2(aw[1] + 24576, aw[1] + 25088);
  acc[1][0] = mfma16(A1[1], X0[1][0], acc[1][0]);
  acc[1][1] = mfma16(A1[1], X0[1][1], acc[1][1]);
  cs[1] = mfma16(A1[1], ones, cs[1]);
  DMA16(m0w + 12288, vw, rw1, 884736);
  SCHED0();
  A0[2] = trrd2(aw[2] + 24576, aw[2] + 25088);
  A0[3] = trrd2(aw[3] + 24576, aw[3] + 25088);
  A0[4] = trrd2(aw[4] + 24576, aw[4] + 25088);
  acc[2][0] = mfma16(A1[2], X0[1][0], acc[2][0]);
  acc[2][1] = mfma16(A1[2], X0[1][1], acc[2][1]);
  cs[2] = mfma16(A1[2], ones, cs[2]);
  SCHED0();
  A0[5] = trrd2(aw[5] + 24576, aw[5] + 25088);
  acc[3][0] = mfma16(A1[3], X0[1][0], acc[3][0]);
  acc[3][1] = mfma16(A1[3], X0[1][1], acc[3][1]);
  DMA16(m0w + 16384, vw, rw1, 884864);
  SCHED0();
  acc[4][0] = mfma16(A1[4], X0[1][0], acc[4][0]);
  acc[4][1] = mfma16(A1[4], X0[1][1], acc[4][1]);
  SCHED0();
  acc[5][0] = mfma16(A1[5], X0[1][0], acc[5][0]);
  acc[5][1] = mfma16(A1[5], X0[1][1], acc[5][1]);
  DMA16(m0w + 20480, vw, rw1, 884992);
  SCHED0();
  WAITVM(8);
  WAITLGKM0B();
  BAR();
  STAMPH(15);
  SCHED0();
  A1[0] = trrd2(aw[0] + 36864, aw[0] + 37376);
  A1[1] = trrd2(aw[1] + 36864, aw[1] + 37376);
  acc[0][0] = mfma16(A0[0], X1[0][0], acc[0][0]);
  acc[0][1] = mfma16(A0[0], X1[0][1], acc[0][1]);
  cs[0] = mfma16(A0[0], ones, cs[0]);
  SCHED0();
  A1[2] = trrd2(aw[2] + 36864, aw[2] + 37376);
  A1[3] = trrd2(aw[3] + 36864, aw[3] + 37376);
  acc[1][0] = mfma16(A0[1], X1[0][0], acc[1][0]);
  acc[1][1] = mfma16(A0[1], X1[0][1], acc[1][1]);
  cs[1] = mfma16(A0[1], ones, cs[1]);
  DMA16(m0x + 8192, vx[0], rx, 640);
  SCHED0();
  A1[4] = trrd2(aw[4] + 36864, aw[4] + 37376);
  A1[5] = trrd2(aw[5] + 36864, aw[5] + 37376);
  acc[2][0] = mfma16(A0[2], X1[0][0], acc[2][0]);
  acc[2][1] = mfma16(A0[2], X1[0][1], acc[2][1]);
  cs[2] = mfma16(A0[2], ones, cs[2]);
  DMA16(m0x + 9216, vx[1], rx, 640);
  SCHED0();
  acc[3][0] = mfma16(A0[3], X1[0][0], acc[3][0]);
  acc[3][1] = mfma16(A0[3], X1[0][1], acc[3][1]);
  DMA16(m0w + 24576, vw, rw1, 983040);
  SCHED0();
  acc[4][0] = mfma16(A0[4], X1[0][0], acc[4][0]);
  acc[4][1] = mfma16(A0[4], X1[0][1], acc[4][1]);
  DMA16(m0w + 28672, vw, rw1, 983168);
  SCHED0();
  acc[5][0] = mfma16(A0[5], X1[0][0], acc[5][0]);
  acc[5][1] = mfma16(A0[5], X1[0][1], acc[5][1]);
  DMA16(m0w + 32768, vw, rw1, 983296);
  SCHED0();
  WAITVM(8);
  WAITLGKM0B();
  BAR();
  SCHED0();
  X0[0][0] = *(LAS u32x4*)(ax[0] + 0);
  X0[0][1] = *(LAS u32x4*)(ax[0] + 2048);
  X0[1][0] = *(LAS u32x4*)(ax[1] + 0);
  acc[0][0] = mfma16(A1[0], X1[1][0], acc[0][0]);
  acc[0][1] = mfma16(A1[0], X1[1][1], acc[0][1]);
  cs[0] = mfma16(A1[0], ones, cs[0]);
  SCHED0();
  X0[1][1] = *(LAS u32x4*)(ax[1] + 2048);
  A0[0] = trrd2(aw[0] + 0, aw[0] + 512);
  A0[1] = trrd2(aw[1] + 0, aw[1] + 512);
  acc[1][0] = mfma16(A1[1], X1[1][0], acc[1][0]);
  acc[1][1] = mfma16(A1[1], X1[1][1], acc[1][1]);
  cs[1] = mfma16(A1[1], ones, cs[1]);
  DMA16(m0w + 36864, vw, rw1, 1081344);
  SCHED0();
  A0[2] = trrd2(aw[2] + 0, aw[2] + 512);
  A0[3] = trrd2(aw[3] + 0, aw[3] + 512);
  A0[4] = trrd2(aw[4] + 0, aw[4] + 512);
  acc[2][0] = mfma16(A1[2], X1[1][0], acc[2][0]);
  acc[2][1] = mfma16(A1[2], X1[1][1], acc[2][1]);
  cs[2] = mfma16(A1[2], ones, cs[2]);
  SCHED0();
  A0[5] = trrd2(aw[5] + 0, aw[5] + 512);
  acc[3][0] = mfma16(A1[3], X1[1][0], acc[3][0]);
  acc[3][1] = mfma16(A1[3], X1[1][1], acc[3][1]);
  DMA16(m0w + 40960, vw, rw1, 1081472);
  SCHED0();
  acc[4][0] = mfma16(A1[4], X1[1][0], acc[4][0]);
  acc[4][1] = mfma16(A1[4], X1[1][1], acc[4][1]);
  SCHED0();
  acc[5][0] = mfma16(A1[5], X1[1][0], acc[5][0]);
  acc[5][1] = mfma16(A1[5], X1[1][1], acc[5][1]);
  DMA16(m0w + 45056, vw, rw1, 1081600);
  SCHED0();
  WAITVM(8);
  WAITLGKM0B();
  BAR();
  STAMPH(16);
  SCHED0();
  A1[0] = trrd2(aw[0] + 12288, aw[0] + 12800);
  A1[1] = trrd2(aw[1] + 12288, aw[1] + 12800);
  acc[0][0] = mfma16(A0[0], X0[0][0], acc[0][0]);
  acc[0][1] = mfma16(A0[0], X0[0][1], acc[0][1]);
  cs[0] = mfma16(A0[0], ones, cs[0]);
  SCHED0();
  A1[2] = trrd2(aw[2] + 12288, aw[2] + 12800);
  A1[3] = trrd2(aw[3] + 12288, aw[3] + 12800);
  acc[1][0] = mfma16(A0[1], X0[0][0], acc[1][0]);
  acc[1][1] = mfma16(A0[1], X0[0][1], acc[1][1]);
  cs[1] = mfma16(A0[1], ones, cs[1]);
  DMA16(m0x + 0, vx[0], rx, 768);
  SCHED0();
  A1[4] = trrd2(aw[4] + 12288, aw[4] + 12800);
  A1[5] = trrd2(aw[5] + 12288, aw[5] + 12800);
  acc[2][0] = mfma16(A0[2], X0[0][0], acc[2][0]);
  acc[2][1] = mfma16(A0[2], X0[0][1], acc[2][1]);
  cs[2] = mfma16(A0[2], ones, cs[2]);
  DMA16(m0x + 1024, vx[1], rx, 768);
  SCHED0();
  acc[3][0] = mfma16(A0[3], X0[0][0], acc[3][0]);
  acc[3][1] = mfma16(A0[3], X0[0][1], acc[3][1]);
  DMA16(m0w + 0, vw, rw1, 1179648);
  SCHED0();
  acc[4][0] = mfma16(A0[4], X0[0][0], acc[4][0]);
  acc[4][1] = mfma16(A0[4], X0[0][1], acc[4][1]);
  DMA16(m0w + 4096, vw, rw1, 1179776);
  SCHED0();
  acc[5][0] = mfma16(A0[5], X0[0][0], acc[5][0]);
  acc[5][1] = mfma16(A0[5], X0[0][1], acc[5][1]);
  DMA16(m0w + 8192, vw, rw1, 1179904);
  SCHED0();
  WAITVM(8);
  WAITLGKM0B();
  BAR();
  SCHED0();
  X1[0][0] = *(LAS u32x4*)(ax[0] + 8192);
  X1[0][1] = *(LAS u32x4*)(ax[0] + 10240);
  X1[1][0] = *(LAS u32x4*)(ax[1] + 8192);
  acc[0][0] = mfma16(A1[0], X0[1][0], acc[0][0]);
  acc[0][1] = mfma16(A1[0], X0[1][1], acc[0][1]);
  cs[0] = mfma16(A1[0], ones, cs[0]);
  SCHED0();
  X1[1][1] = *(LAS u32x4*)(ax[1] + 10240);
  A0[0] = trrd2(aw[0] + 24576, aw[0] + 25088);
  A0[1] = trrd2(aw[1] + 24576, aw[1] + 25088);
  acc[1][0] = mfma16(A1[1], X0[1][0], acc[1][0]);
  acc[1][1] = mfma16(A1[1], X0[1][1], acc[1][1]);
  cs[1] = mfma16(A1[1], ones, cs[1]);
  DMA16(m0w + 12288, vw, rw1, 1277952);
  SCHED0();
  A0[2] = trrd2(aw[2] + 24576, aw[2] + 25088);
  A0[3] = trrd2(aw[3] + 24576, aw[3] + 25088);
  A0[4] = trrd2(aw[4] + 24576, aw[4] + 25088);
  acc[2][0] = mfma16(A1[2], X0[1][0], acc[2][0]);
  acc[2][1] = mfma16(A1[2], X0[1][1], acc[2][1]);
  cs[2] = mfma16(A1[2], ones, cs[2]);
  SCHED0();
  A0[5] = trrd2(aw[5] + 24576, aw[5] + 25088);
  acc[3][0] = mfma16(A1[3], X0[1][0], acc[3][0]);
  acc[3][1] = mfma16(A1[3], X0[1][1], acc[3][1]);
  DMA16(m0w + 16384, vw, rw1, 1278080);
  SCHED0();
  acc[4][0] = mfma16(A1[4], X0[1][0], acc[4][0]);
  acc[4][1] = mfma16(A1[4], X0[1][1], acc[4][1]);
  SCHED0();
  acc[5][0] = mfma16(A1[5], X0[1][0], acc[5][0]);
  acc[5][1] = mfma16(A1[5], X0[1][1], acc[5][1]);
  DMA16(m0w + 20480, vw, rw1, 1278208);
  SCHED0();
  WAITVM(8);
  WAITLGKM0B();
  BAR();
  STAMPH(17);
  SCHED0();
  A1[0] = trrd2(aw[0] + 36864, aw[0] + 37376);
  A1[1] = trrd2(aw[1] + 36864, aw[1] + 37376);
  acc[0][0] = mfma16(A0[0], X1[0][0], acc[0][0]);
  acc[0][1] = mfma16(A0[0], X1[0][1], acc[0][1]);
  cs[0] = mfma16(A0[0], ones, cs[0]);
  SCHED0();
  A1[2] = trrd2(aw[2] + 36864, aw[2] + 37376);
  A1[3] = trrd2(aw[3] + 36864, aw[3] + 37376);
  acc[1][0] = mfma16(A0[1], X1[0][0], acc[1][0]);
  acc[1][1] = mfma16(A0[1], X1[0][1], acc[1][1]);
  cs[1] = mfma16(A0[1], ones, cs[1]);
  DMA16(m0x + 8192, vx[0], rx, 896);
  SCHED0();
  A1[4] = trrd2(aw[4] + 36864, aw[4] + 37376);
  A1[5] = trrd2(aw[5] + 36864, aw[5] + 37376);
  acc[2][0] = mfma16(A0[2], X1[0][0], acc[2][0]);
  acc[2][1] = mfma16(A0[2], X1[0][1], acc[2][1]);
  cs[2] = mfma16(A0[2], ones, cs[2]);
  DMA16(m0x + 9216, vx[1], rx, 896);
  SCHED0();
  acc[3][0] = mfma16(A0[3], X1[0][0], acc[3][0]);
  acc[3][1] = mfma16(A0[3], X1[0][1], acc[3][1]);
  DMA16(m0w + 24576, vw, rw1, 1376256);
  SCHED0();
  acc[4][0] = mfma16(A0[4], X1[0][0], acc[4][0]);
  acc[4][1] = mfma16(A0[4], X1[0][1], acc[4][1]);
  DMA16(m0w + 28672, vw, rw1, 1376384);
  SCHED0();
  acc[5][0] = mfma16(A0[5], X1[0][0], acc[5][0]);
  acc[5][1] = mfma16(A0[5], X1[0][1], acc[5][1]);
  DMA16(m0w + 32768, vw, rw1, 1376512);
  SCHED0();
  WAITVM(8);
  WAITLGKM0B();
  BAR();
  SCHED0();
  X0[0][0] = *(LAS u32x4*)(ax[0] + 0);
  X0[0][1] = *(LAS u32x4*)(ax[0] + 2048);
  X0[1][0] = *(LAS u32x4*)(ax[1] + 0);
  acc[0][0] = mfma16(A1[0], X1[1][0], acc[0][0]);
  acc[0][1] = mfma16(A1[0], X1[1][1], acc[0][1]);
  cs[0] = mfma16(A1[0], ones, cs[0]);
  SCHED0();
  X0[1][1] = *(LAS u32x4*)(ax[1] + 2048);
  A0[0] = trrd2(aw[0] + 0, aw[0] + 512);
  A0[1] = trrd2(aw[1] + 0, aw[1] + 512);
  acc[1][0] = mfma16(A1[1], X1[1][0], acc[1][0]);
  acc[1][1] = mfma16(A1[1], X1[1][1], acc[1][1]);
  cs[1] = mfma16(A1[1], ones, cs[1]);
  DMA16(m0w + 36864, vw, rw1, 1474560);
  SCHED0();
  A0[2] = trrd2(aw[2] + 0, aw[2] + 512);
  A0[3] = trrd2(aw[3] + 0, aw[3] + 512);
  A0[4] = trrd2(aw[4] + 0, aw[4] + 512);
  acc[2][0] = mfma16(A1[2], X1[1][0], acc[2][0]);
  acc[2][1] = mfma16(A1[2], X1[1][1], acc[2][1]);
  cs[2] = mfma16(A1[2], ones, cs[2]);
  SCHED0();
  A0[5] = trrd2(aw[5] + 0, aw[5] + 512);
  acc[3][0] = mfma16(A1[3], X1[1][0], acc[3][0]);
  acc[3][1] = mfma16(A1[3], X1[1][1], acc[3][1]);
  DMA16(m0w + 40960, vw, rw1, 1474688);
  SCHED0();
  acc[4][0] = mfma16(A1[4], X1[1][0], acc[4][0]);
  acc[4][1] = mfma16(A1[4], X1[1][1], acc[4][1]);
  SCHED0();
  acc[5][0] = mfma16(A1[5], X1[1][0], acc[5][0]);
  acc[5][1] = mfma16(A1[5], X1[1][1], acc[5][1]);
  DMA16(m0w + 45056, vw, rw1, 1474816);
  SCHED0();
  WAITVM(8);
  WAITLGKM0B();
  BAR();
  STAMPH(18);
  SCHED0();
  A1[0] = trrd2(aw[0] + 12288, aw[0] + 12800);
  A1[1] = trrd2(aw[1] + 12288, aw[1] + 12800);
  acc[0][0] = mfma16(A0[0], X0[0][0], acc[0][0]);
  acc[0][1] = mfma16(A0[0], X0[0][1], acc[0][1]);
  cs[0] = mfma16(A0[0], ones, cs[0]);
  SCHED0();
  A1[2] = trrd2(aw[2] + 12288, aw[2] + 12800);
  A1[3] = trrd2(aw[3] + 12288, aw[3] + 12800);
  acc[1][0] = mfma16(A0[1], X0[0][0], acc[1][0]);
  acc[1][1] = mfma16(A0[1], X0[0][1], acc[1][1]);
  cs[1] = mfma16(A0[1], ones, cs[1]);
  DMA16(m0x + 0, vx[0], rx, 1024);
  SCHED0();
  A1[4] = trrd2(aw[4] + 12288, aw[4] + 12800);
  A1[5] = trrd2(aw[5] + 12288, aw[5] + 12800);
  acc[2][0] = mfma16(A0[2], X0[0][0], acc[2][0]);
  acc[2][1] = mfma16(A0[2], X0[0][1], acc[2][1]);
  cs[2] = mfma16(A0[2], ones, cs[2]);
  DMA16(m0x + 1024, vx[1], rx, 1024);
  SCHED0();
  acc[3][0] = mfma16(A0[3], X0[0][0], acc[3][0]);
  acc[3][1] = mfma16(A0[3], X0[0][1], acc[3][1]);
  DMA16(m0w + 0, vw, rw1, 1572864);
  SCHED0();
  acc[4][0] = mfma16(A0[4], X0[0][0], acc[4][0]);
  acc[4][1] = mfma16(A0[4], X0[0][1], acc[4][1]);
  DMA16(m0w + 4096, vw, rw1, 1572992);
  SCHED0();
  acc[5][0] = mfma16(A0[5], X0[0][0], acc[5][0]);
  acc[5][1] = mfma16(A0[5], X0[0][1], acc[5][1]);
  DMA16(m0w + 8192, vw, rw1, 1573120);
  LD32SC1(w2fv, vw2f, rws, 0);
  SCHED0();
  WAITVM(8);
  WAITLGKM0B();
  BAR();
  SCHED0();
  X1[0][0] = *(LAS u32x4*)(ax[0] + 8192);
  X1[0][1] = *(LAS u32x4*)(ax[0] + 10240);
  X1[1][0] = *(LAS u32x4*)(ax[1] + 8192);
  acc[0][0] = mfma16(A1[0], X0[1][0], acc[0][0]);
  acc[0][1] = mfma16(A1[0], X0[1][1], acc[0][1]);
  cs[0] = mfma16(A1[0], ones, cs[0]);
  SCHED0();
  X1[1][1] = *(LAS u32x4*)(ax[1] + 10240);
  A0[0] = trrd2(aw[0] + 24576, aw[0] + 25088);
  A0[1] = trrd2(aw[1] + 24576, aw[1] + 25088);
  acc[1][0] = mfma16(A1[1], X0[1][0], acc[1][0]);
  acc[1][1] = mfma16(A1[1], X0[1][1], acc[1][1]);
  cs[1] = mfma16(A1[1], ones, cs[1]);
  DMA16(m0w + 12288, vw, rw1, 1671168);
  SCHED0();
  A0[2] = trrd2(aw[2] + 24576, aw[2] + 25088);
  A0[3] = trrd2(aw[3] + 24576, aw[3] + 25088);
  A0[4] = trrd2(aw[4] + 24576, aw[4] + 25088);
  acc[2][0] = mfma16(A1[2], X0[1][0], acc[2][0]);
  acc[2][1] = mfma16(A1[2], X0[1][1], acc[2][1]);
  cs[2] = mfma16(A1[2], ones, cs[2]);
  SCHED0();
  A0[5] = trrd2(aw[5] + 24576, aw[5] + 25088);
  acc[3][0] = mfma16(A1[3], X0[1][0], acc[3][0]);
  acc[3][1] = mfma16(A1[3], X0[1][1], acc[3][1]);
  DMA16(m0w + 16384, vw, rw1, 1671296);
  SCHED0();
  acc[4][0] = mfma16(A1[4], X0[1][0], acc[4][0]);
  acc[4][1] = mfma16(A1[4], X0[1][1], acc[4][1]);
  SCHED0();
  acc[5][0] = mfma16(A1[5], X0[1][0], acc[5][0]);
  acc[5][1] = mfma16(A1[5], X0[1][1], acc[5][1]);
  DMA16(m0w + 20480, vw, rw1, 1671424);
  SCHED0();
  WAITVM(8);
  WAITLGKM0B();
  BAR();
  STAMPH(19);
  SCHED0();
  A1[0] = trrd2(aw[0] + 36864, aw[0] + 37376);
  A1[1] = trrd2(aw[1] + 36864, aw[1] + 37376);
  acc[0][0] = mfma16(A0[0], X1[0][0], acc[0][0]);
  acc[0][1] = mfma16(A0[0], X1[0][1], acc[0][1]);
  cs[0] = mfma16(A0[0], ones, cs[0]);
  SCHED0();
  A1[2] = trrd2(aw[2] + 36864, aw[2] + 37376);
  A1[3] = trrd2(aw[3] + 36864, aw[3] + 37376);
  acc[1][0] = mfma16(A0[1], X1[0][0], acc[1][0]);
  acc[1][1] = mfma16(A0[1], X1[0][1], acc[1][1]);
  cs[1] = mfma16(A0[1], ones, cs[1]);
  DMA16(m0x + 8192, vx[0], rx, 1152);
  SCHED0();
  A1[4] = trrd2(aw[4] + 36864, aw[4] + 37376);
  A1[5] = trrd2(aw[5] + 36864, aw[5] + 37376);
  acc[2][0] = mfma16(A0[2], X1[0][0], acc[2][0]);
  acc[2][1] = mfma16(A0[2], X1[0][1], acc[2][1]);
  cs[2] = mfma16(A0[2], ones, cs[2]);
  DMA16(m0x + 9216, vx[1], rx, 1152);
  SCHED0();
  acc[3][0] = mfma16(A0[3], X1[0][0], acc[3][0]);
  acc[3][1] = mfma16(A0[3], X1[0][1], acc[3][1]);
  DMA16(m0w + 24576, vw, rw1, 1769472);
  SCHED0();
  acc[4][0] = mfma16(A0[4], X1[0][0], acc[4][0]);
  acc[4][1] = mfma16(A0[4], X1[0][1], acc[4][1]);
  DMA16(m0w + 28672, vw, rw1, 1769600);
  SCHED0();
  acc[5][0] = mfma16(A0[5], X1[0][0], acc[5][0]);
  acc[5][1] = mfma16(A0[5], X1[0][1], acc[5][1]);
  DMA16(m0w + 32768, vw, rw1, 1769728);
  SCHED0();
  WAITVM(8);
  WAITLGKM0B();
  BAR();
  PIN1(w2fv);
  if (!__all(w2fv == tag)) poll_flag(vw2f, rws, tag);
  STAMPR(2);
  SCHED0();
  X0[0][0] = *(LAS u32x4*)(ax[0] + 0);
  X0[0][1] = *(LAS u32x4*)(ax[0] + 2048);
  X0[1][0] = *(LAS u32x4*)(ax[1] + 0);
  acc[0][0] = mfma16(A1[0], X1[1][0], acc[0][0]);
  acc[0][1] = mfma16(A1[0], X1[1][1], acc[0][1]);
  cs[0] = mfma16(A1[0], ones, cs[0]);
  DMA16(m0w + 36864, vw, rw1, 1867776);
  SCHED0();
  X0[1][1] = *(LAS u32x4*)(ax[1] + 2048);
  A0[0] = trrd2(aw[0] + 0, aw[0] + 512);
  A0[1] = trrd2(aw[1] + 0, aw[1] + 512);
  acc[1][0] = mfma16(A1[1], X1[1][0], acc[1][0]);
  acc[1][1] = mfma16(A1[1], X1[1][1], acc[1][1]);
  cs[1] = mfma16(A1[1], ones, cs[1]);
  DMA16(m0w + 40960, vw, rw1, 1867904);
  SCHED0();
  A0[2] = trrd2(aw[2] + 0, aw[2] + 512);
  A0[3] = trrd2(aw[3] + 0, aw[3] + 512);
  A0[4] = trrd2(aw[4] + 0, aw[4] + 512);
  acc[2][0] = mfma16(A1[2], X1[1][0], acc[2][0]);
  acc[2][1] = mfma16(A1[2], X1[1][1], acc[2][1]);
  cs[2] = mfma16(A1[2], ones, cs[2]);
  DMA16(m0w + 45056, vw, rw1, 1868032);
  SCHED0();
  A0[5] = trrd2(aw[5] + 0, aw[5] + 512);
  acc[3][0] = mfma16(A1[3], X1[1][0], acc[3][0]);
  acc[3][1] = mfma16(A1[3], X1[1][1], acc[3][1]);
  DMA16S(m0p + 49152, vl16, rws, w2base + 0, 0);
  SCHED0();
  acc[4][0] = mfma16(A1[4], X1[1][0], acc[4][0]);
  acc[4][1] = mfma16(A1[4], X1[1][1], acc[4][1]);
  DMA16S(m0p + 49152, vl16, rws, w2base + 0, 1024);
  SCHED0();
  acc[5][0] = mfma16(A1[5], X1[1][0], acc[5][0]);
  acc[5][1] = mfma16(A1[5], X1[1][1], acc[5][1]);
  DMA16S(m0p + 49152, vl16, rws, w2base + 0, 2048);
  SCHED0();
  WAITVM(11);
  WAITLGKM0B();
  BAR();
  STAMPH(20);
  SCHED0();
  A1[0] = trrd2(aw[0] + 12288, aw[0] + 12800);
  A1[1] = trrd2(aw[1] + 12288, aw[1] + 12800);
  acc[0][0] = mfma16(A0[0], X0[0][0], acc[0][0]);
  acc[0][1] = mfma16(A0[0], X0[0][1], acc[0][1]);
  cs[0] = mfma16(A0[0], ones, cs[0]);
  SCHED0();
  A1[2] = trrd2(aw[2] + 12288, aw[2] + 12800);
  A1[3] = trrd2(aw[3] + 12288, aw[3] + 12800);
  acc[1][0] = mfma16(A0[1], X0[0][0], acc[1][0]);
  acc[1][1] = mfma16(A0[1], X0[0][1], acc[1][1]);
  cs[1] = mfma16(A0[1], ones, cs[1]);
  DMA16S(m0x + 0, vl16, rws, w2base + 3072, 0);
  SCHED0();
  A1[4] = trrd2(aw[4] + 12288, aw[4] + 12800);
  A1[5] = trrd2(aw[5] + 12288, aw[5] + 12800);
  acc[2][0] = mfma16(A0[2], X0[0][0], acc[2][0]);
  acc[2][1] = mfma16(A0[2], X0[0][1], acc[2][1]);
  cs[2] = mfma16(A0[2], ones, cs[2]);
  DMA16S(m0x + 0, vl16, rws, w2base + 3072, 1024);
  SCHED0();
  acc[3][0] = mfma16(A0[3], X0[0][0], acc[3][0]);
  acc[3][1] = mfma16(A0[3], X0[0][1], acc[3][1]);
  DMA16S(m0p + 0, vl16, rws, w2base + 5120, 0);
  SCHED0();
  acc[4][0] = mfma16(A0[4], X0[0][0], acc[4][0]);
  acc[4][1] = mfma16(A0[4], X0[0][1], acc[4][1]);
  DMA16S(m0p + 0, vl16, rws, w2base + 5120, 1024);
  SCHED0();
  acc[5][0] = mfma16(A0[5], X0[0][0], acc[5][0]);
  acc[5][1] = mfma16(A0[5], X0[0][1], acc[5][1]);
  DMA16S(m0p + 0, vl16, rws, w2base + 5120, 2048);
  LD64SC1(stt[0], vst, rws, 0, 0);
  LD64SC1(stt[1], vst, rws, 0, 128);
  SCHED0();
  WAITVM(13);
  WAITLGKM0B();
  BAR();
  SCHED0();
  X1[0][0] = *(LAS u32x4*)(ax[0] + 8192);
  X1[0][1] = *(LAS u32x4*)(ax[0] + 10240);
  X1[1][0] = *(LAS u32x4*)(ax[1] + 8192);
  acc[0][0] = mfma16(A1[0], X0[1][0], acc[0][0]);
  acc[0][1] = mfma16(A1[0], X0[1][1], acc[0][1]);
  cs[0] = mfma16(A1[0], ones, cs[0]);
  SCHED0();
  X1[1][1] = *(LAS u32x4*)(ax[1] + 10240);
  A0[0] = trrd2(aw[0] + 24576, aw[0] + 25088);
  A0[1] = trrd2(aw[1] + 24576, aw[1] + 25088);
  acc[1][0] = mfma16(A1[1], X0[1][0], acc[1][0]);
  acc[1][1] = mfma16(A1[1], X0[1][1], acc[1][1]);
  cs[1] = mfma16(A1[1], ones, cs[1]);
  DMA16S(m0p + 12288, vl16, rws, w2base + 8192, 0);
  SCHED0();
  A0[2] = trrd2(aw[2] + 24576, aw[2] + 25088);
  A0[3] = trrd2(aw[3] + 24576, aw[3] + 25088);
  A0[4] = trrd2(aw[4] + 24576, aw[4] + 25088);
  acc[2][0] = mfma16(A1[2], X0[1][0], acc[2][0]);
  acc[2][1] = mfma16(A1[2], X0[1][1], acc[2][1]);
  cs[2] = mfma16(A1[2], ones, cs[2]);
  SCHED0();
  A0[5] = trrd2(aw[5] + 24576, aw[5] + 25088);
  acc[3][0] = mfma16(A1[3], X0[1][0], acc[3][0]);
  acc[3][1] = mfma16(A1[3], X0[1][1], acc[3][1]);
  DMA16S(m0p + 12288, vl16, rws, w2base + 8192, 1024);
  SCHED0();
  acc[4][0] = mfma16(A1[4], X0[1][0], acc[4][0]);
  acc[4][1] = mfma16(A1[4], X0[1][1], acc[4][1]);
  SCHED0();
  acc[5][0] = mfma16(A1[5], X0[1][0], acc[5][0]);
  acc[5][1] = mfma16(A1[5], X0[1][1], acc[5][1]);
  DMA16S(m0p + 12288, vl16, rws, w2base + 8192, 2048);
  SCHED0();
  WAITVM(13);
  WAITLGKM0B();
  BAR();
  STAMPH(21);
  SCHED0();
  A1[0] = trrd2(aw[0] + 36864, aw[0] + 37376);
  A1[1] = trrd2(aw[1] + 36864, aw[1] + 37376);
  acc[0][0] = mfma16(A0[0], X1[0][0], acc[0][0]);
  acc[0][1] = mfma16(A0[0], X1[0][1], acc[0][1]);
  cs[0] = mfma16(A0[0], ones, cs[0]);
  SCHED0();
  A1[2] = trrd2(aw[2] + 36864, aw[2] + 37376);
  A1[3] = trrd2(aw[3] + 36864, aw[3] + 37376);
  acc[1][0] = mfma16(A0[1], X1[0][0], acc[1][0]);
  acc[1][1] = mfma16(A0[1], X1[0][1], acc[1][1]);
  cs[1] = mfma16(A0[1], ones, cs[1]);
  DMA16S(m0x + 8192, vl16, rws, w2base + 11264, 0);
  SCHED0();
  A1[4] = trrd2(aw[4] + 36864, aw[4] + 37376);
  A1[5] = trrd2(aw[5] + 36864, aw[5] + 37376);
  acc[2][0] = mfma16(A0[2], X1[0][0], acc[2][0]);
  acc[2][1] = mfma16(A0[2], X1[0][1], acc[2][1]);
  cs[2] = mfma16(A0[2], ones, cs[2]);
  DMA16S(m0x + 8192, vl16, rws, w2base + 11264, 1024);
  SCHED0();
  acc[3][0] = mfma16(A0[3], X1[0][0], acc[3][0]);
  acc[3][1] = mfma16(A0[3], X1[0][1], acc[3][1]);
  DMA16S(m0p + 24576, vl16, rws, w2base + 13312, 0);
  SCHED0();
  acc[4][0] = mfma16(A0[4], X1[0][0], acc[4][0]);
  acc[4][1] = mfma16(A0[4], X1[0][1], acc[4][1]);
  DMA16S(m0p + 24576, vl16, rws, w2base + 13312, 1024);
  SCHED0();
  acc[5][0] = mfma16(A0[5], X1[0][0], acc[5][0]);
  acc[5][1] = mfma16(A0[5], X1[0][1], acc[5][1]);
  DMA16S(m0p + 24576, vl16, rws, w2base + 13312, 2048);
  SCHED0();
  WAITLGKM0B();
  BAR();
  SCHED0();
  acc[0][0] = mfma16(A1[0], X1[1][0], acc[0][0]);
  acc[0][1] = mfma16(A1[0], X1[1][1], acc[0][1]);
  cs[0] = mfma16(A1[0], ones, cs[0]);
  SCHED0();
  acc[1][0] = mfma16(A1[1], X1[1][0], acc[1][0]);
  acc[1][1] = mfma16(A1[1], X1[1][1], acc[1][1]);
  cs[1] = mfma16(A1[1], ones, cs[1]);
  DMA16S(m0p + 36864, vl16, rws, w2base + 16384, 0);
  SCHED0();
  acc[2][0] = mfma16(A1[2], X1[1][0], acc[2][0]);
  acc[2][1] = mfma16(A1[2], X1[1][1], acc[2][1]);
  cs[2] = mfma16(A1[2], ones, cs[2]);
  SCHED0();
  acc[3][0] = mfma16(A1[3], X1[1][0], acc[3][0]);
  acc[3][1] = mfma16(A1[3], X1[1][1], acc[3][1]);
  DMA16S(m0p + 36864, vl16, rws, w2base + 16384, 1024);
  SCHED0();
  acc[4][0] = mfma16(A1[4], X1[1][0], acc[4][0]);
  acc[4][1] = mfma16(A1[4], X1[1][1], acc[4][1]);
  SCHED0();
  acc[5][0] = mfma16(A1[5], X1[1][0], acc[5][0]);
  acc[5][1] = mfma16(A1[5], X1[1][1], acc[5][1]);
  DMA16S(m0p + 36864, vl16, rws, w2base + 16384, 2048);
  SCHED0();
  {
    LAS char* ex = L + 77824;
    if (m16 == 0) {
#pragma unroll
      for (int f = 0; f < 3; ++f) *(LAS f32x4*)(ex + w * 192 + f * 64 + g * 16) = cs[f];
    }
    WAITLGKM0B();
    BAR();
#pragma unroll
    for (int f = 3; f < 6; ++f) cs[f] = *(LAS f32x4*)(ex + (w ^ 1) * 192 + (5 - f) * 64 + g * 16);
  }
  asm volatile("s_waitcnt vmcnt(11)"
               : "+v"(stt[0]), "+v"(stt[1]), "+v"(b1r[0]), "+v"(b1r[1]), "+v"(b1r[2]), "+v"(b1r[3]), "+v"(b1r[4]), "+v"(b1r[5])::"memory");
  STAMPR(3);
  if (!__all(stt[0].y == tag && stt[1].y == tag)) {
    _Pragma("unroll 1") for (int sp = 0; sp < SPIN_MAX; ++sp) {
      asm volatile("buffer_load_dwordx2 %0, %2, %3, 0 offen sc1\n\tbuffer_load_dwordx2 %1, %2, %3, 0 offen offset:128 sc1\n\ts_waitcnt vmcnt(0)"
                   : "=&v"(stt[0]), "=&v"(stt[1]) : "v"(vst), "s"(rws) : "memory");
      if (__all(stt[0].y == tag && stt[1].y == tag)) break;
      __builtin_amdgcn_s_sleep(1);
    }
  }
  STAMPR(4);
  {
    float rr[2], nm[2];
#pragma unroll
    for (int mf = 0; mf < 2; ++mf) {
      rr[mf] = __builtin_amdgcn_rcpf(bfhi(stt[mf].x));
      nm[mf] = -rr[mf] * bflo(stt[mf].x);
    }
    const float C1 = -2.f * 1.4426950408889634f * 0.7978845608028654f, C2 = C1 * 0.044715f;
    const u32 hbase = WS_HP + ((ti * 48 + 6 * tj + 3 * wn) * 4 + 2 * wm) * 1024;
#pragma unroll
    for (int pp = 0; pp < 3; ++pp) {
      const u32 hoff = hbase + (wm ? 2 - pp : pp) * 4096;
#pragma unroll
      for (int mf = 0; mf < 2; ++mf) {
        u32 hq[4];
#pragma unroll
        for (int hf = 0; hf < 2; ++hf) {
          const int f = 2 * pp + hf;
#pragma unroll
          for (int e = 0; e < 2; ++e) {
            const u32 bw = b1r[f][e];
            f32x2 bb = {bflo(bw), bfhi(bw)};
            f32x2 cc = {cs[f][2 * e], cs[f][2 * e + 1]};
            f32x2 aa = {acc[f][mf][2 * e], acc[f][mf][2 * e + 1]};
            f32x2 h = aa * rr[mf] + (cc * nm[mf] + bb);
            f32x2 z = h * (h * h * C2 + C1);
            f32x2 dd = {__builtin_amdgcn_exp2f(z.x), __builtin_amdgcn_exp2f(z.y)};
            dd += 1.f;
            f32x2 rc = {__builtin_amdgcn_rcpf(dd.x), __builtin_amdgcn_rcpf(dd.y)};
            h *= rc;
            hq[2 * hf + e] = pkbf(h.x, h.y);
          }
        }
        const u32x4 hv = {wm ? hq[2] : hq[0], wm ? hq[3] : hq[1], wm ? hq[0] : hq[2], wm ? hq[1] : hq[3]};
        if (mf == 0) ST128SC1(hv, vl16, rws, hoff, 0);
        else ST128SC1(hv, vl16, rws, hoff, 1024);
      }
    }
  }
  STAMPR(5);
  u32x4 W19, Wr[4][5], H[3][4];
  LD128SC1(W19, vl16, rws, w2base + 19 * 1024, 0);
#define LDW(SS)                                                           \
  LD128SC1(Wr[(SS) & 3][0], vl16, rws, w2base + (SS) * 5120, 0);          \
  LD128SC1(Wr[(SS) & 3][1], vl16, rws, w2base + (SS) * 5120, 1024);       \
  LD128SC1(Wr[(SS) & 3][2], vl16, rws, w2base + (SS) * 5120, 2048);       \
  LD128SC1(Wr[(SS) & 3][3], vl16, rws, w2base + (SS) * 5120, 3072);       \
  LD128SC1(Wr[(SS) & 3][4], vl16, rws, w2base + (SS) * 5120 + 4096, 0);
  LDW(4) LDW(5) LDW(6) LDW(7)
  WAITVM(21);
  BAR();
  if (tid == 0) {
    const u32 vf = WS_HF + (ti * 8 + tj) * 128;
    asm volatile("buffer_store_dword %0, %1, %2, 0 offen sc1" ::"v"(tag), "v"(vf), "s"(rws) : "memory");
  }
  STAMPR(6);
  poll_flag(WS_HF + (ti * 8 + 2 * w + (lane & 1)) * 128, rws, tag);
  asm volatile("" : "+v"(W19), "+v"(Wr[0][0]), "+v"(Wr[0][1]), "+v"(Wr[0][2]), "+v"(Wr[0][3]), "+v"(Wr[0][4]), "+v"(Wr[1][0]), "+v"(Wr[1][1]),
               "+v"(Wr[1][2]), "+v"(Wr[1][3]), "+v"(Wr[1][4]));
  asm volatile("" : "+v"(Wr[2][0]), "+v"(Wr[2][1]), "+v"(Wr[2][2]), "+v"(Wr[2][3]), "+v"(Wr[2][4]), "+v"(Wr[3][0]), "+v"(Wr[3][1]), "+v"(Wr[3][2]),
               "+v"(Wr[3][3]), "+v"(Wr[3][4]));
  STAMPR(7);
  u32 hrd[4];
#pragma unroll
  for (int ml = 0; ml < 4; ++ml) hrd[ml] = WS_HP + (ti * 48 + 12 * w) * 4096 + ((ml + w) & 3) * 1024;
  f32x4 o[5][4];
  LAS char* wl2 = L + w * 2048 + lane * 16;
  LAS char* wl3 = L + 16384 + w * 3072 + lane * 16;
  u32x2 xr[5], b2r[5], sr[5];
  u32 sfv;
  const u32 vxo = (64 * ti + 16 * w + m16) * 1280 + (80 * tj + 4 * g) * 2;
  const u32 vb2 = (80 * tj + 4 * g) * 2;
  const u32 vsf = WS_SF + 128 * tj + 4 * (lane < 20 ? lane : 19);
  const u32 vsp = WS_SP + (20 * tj + g) * 16384 + (64 * ti + 16 * w + m16) * 8;
  LD128SC1(H[0][0], vl16, rws, hrd[0] + 0, 0);
  LD128SC1(H[0][1], vl16, rws, hrd[1] + 0, 0);
  LD128SC1(H[0][2], vl16, rws, hrd[2] + 0, 0);
  LD128SC1(H[0][3], vl16, rws, hrd[3] + 0, 0);
  LD128SC1(H[1][0], vl16, rws, hrd[0] + 4096, 0);
  LD128SC1(H[1][1], vl16, rws, hrd[1] + 4096, 0);
  LD128SC1(H[1][2], vl16, rws, hrd[2] + 4096, 0);
  LD128SC1(H[1][3], vl16, rws, hrd[3] + 4096, 0);
  LD128SC1(H[2][0], vl16, rws, hrd[0] + 8192, 0);
  LD128SC1(H[2][1], vl16, rws, hrd[1] + 8192, 0);
  LD128SC1(H[2][2], vl16, rws, hrd[2] + 8192, 0);
  LD128SC1(H[2][3], vl16, rws, hrd[3] + 8192, 0);
  u32x4 AL[5];
  AL[0] = *(LAS u32x4*)(wl3 + 49152);
  AL[1] = *(LAS u32x4*)(wl3 + 50176);
  AL[2] = *(LAS u32x4*)(wl3 + 51200);
  AL[3] = *(LAS u32x4*)(wl2 + 0);
  AL[4] = *(LAS u32x4*)(wl2 + 1024);
  asm volatile("s_waitcnt vmcnt(11)" : "+v"(H[0][0]) :: "memory");
  o[0][0] = mfma16(AL[0], H[0][0], zero4);
  o[1][0] = mfma16(AL[1], H[0][0], zero4);
  o[2][0] = mfma16(AL[2], H[0][0], zero4);
  o[3][0] = mfma16(AL[3], H[0][0], zero4);
  o[4][0] = mfma16(AL[4], H[0][0], zero4);
  SCHED0();
  LD128SC1(H[0][0], vl16, rws, hrd[0] + 12288, 0);
  asm volatile("s_waitcnt vmcnt(11)" : "+v"(H[0][1]) :: "memory");
  o[0][1] = mfma16(AL[0], H[0][1], zero4);
  o[1][1] = mfma16(AL[1], H[0][1], zero4);
  o[2][1] = mfma16(AL[2], H[0][1], zero4);
  o[3][1] = mfma16(AL[3], H[0][1], zero4);
  o[4][1] = mfma16(AL[4], H[0][1], zero4);
  SCHED0();
  LD128SC1(H[0][1], vl16, rws, hrd[1] + 12288, 0);
  asm volatile("s_waitcnt vmcnt(11)" : "+v"(H[0][2]) :: "memory");
  o[0][2] = mfma16(AL[0], H[0][2], zero4);
  o[1][2] = mfma16(AL[1], H[0][2], zero4);
  o[2][2] = mfma16(AL[2], H[0][2], zero4);
  o[3][2] = mfma16(AL[3], H[0][2], zero4);
  o[4][2] = mfma16(AL[4], H[0][2], zero4);
  SCHED0();
  LD128SC1(H[0][2], vl16, rws, hrd[2] + 12288, 0);
  asm volatile("s_waitcnt vmcnt(11)" : "+v"(H[0][3]) :: "memory");
  o[0][3] = mfma16(AL[0], H[0][3], zero4);
  o[1][3] = mfma16(AL[1], H[0][3], zero4);
  o[2][3] = mfma16(AL[2], H[0][3], zero4);
  o[3][3] = mfma16(AL[3], H[0][3], zero4);
  o[4][3] = mfma16(AL[4], H[0][3], zero4);
  SCHED0();
  LD128SC1(H[0][3], vl16, rws, hrd[3] + 12288, 0);
  AL[0] = *(LAS u32x4*)(wl3 + 0);
  AL[1] = *(LAS u32x4*)(wl3 + 1024);
  AL[2] = *(LAS u32x4*)(wl3 + 2048);
  AL[3] = *(LAS u32x4*)(wl3 + 12288);
  AL[4] = *(LAS u32x4*)(wl3 + 13312);
  asm volatile("s_waitcnt vmcnt(11)" : "+v"(H[1][0]) :: "memory");
  o[0][0] = mfma16(AL[0], H[1][0], o[0][0]);
  o[1][0] = mfma16(AL[1], H[1][0], o[1][0]);
  o[2][0] = mfma16(AL[2], H[1][0], o[2][0]);
  o[3][0] = mfma16(AL[3], H[1][0], o[3][0]);
  o[4][0] = mfma16(AL[4], H[1][0], o[4][0]);
  SCHED0();
  LD128SC1(H[1][0], vl16, rws, hrd[0] + 16384, 0);
  asm volatile("s_waitcnt vmcnt(11)" : "+v"(H[1][1]) :: "memory");
  o[0][1] = mfma16(AL[0], H[1][1], o[0][1]);
  o[1][1] = mfma16(AL[1], H[1][1], o[1][1]);
  o[2][1] = mfma16(AL[2], H[1][1], o[2][1]);
  o[3][1] = mfma16(AL[3], H[1][1], o[3][1]);
  o[4][1] = mfma16(AL[4], H[1][1], o[4][1]);
  SCHED0();
  LD128SC1(H[1][1], vl16, rws, hrd[1] + 16384, 0);
  asm volatile("s_waitcnt vmcnt(11)" : "+v"(H[1][2]) :: "memory");
  o[0][2] = mfma16(AL[0], H[1][2], o[0][2]);
  o[1][2] = mfma16(AL[1], H[1][2], o[1][2]);
  o[2][2] = mfma16(AL[2], H[1][2], o[2][2]);
  o[3][2] = mfma16(AL[3], H[1][2], o[3][2]);
  o[4][2] = mfma16(AL[4], H[1][2], o[4][2]);
  SCHED0();
  LD128SC1(H[1][2], vl16, rws, hrd[2] + 16384, 0);
  asm volatile("s_waitcnt vmcnt(11)" : "+v"(H[1][3]) :: "memory");
  o[0][3] = mfma16(AL[0], H[1][3], o[0][3]);
  o[1][3] = mfma16(AL[1], H[1][3], o[1][3]);
  o[2][3] = mfma16(AL[2], H[1][3], o[2][3]);
  o[3][3] = mfma16(AL[3], H[1][3], o[3][3]);
  o[4][3] = mfma16(AL[4], H[1][3], o[4][3]);
  SCHED0();
  LD128SC1(H[1][3], vl16, rws, hrd[3] + 16384, 0);
  AL[0] = *(LAS u32x4*)(wl3 + 14336);
  AL[1] = *(LAS u32x4*)(wl2 + 8192);
  AL[2] = *(LAS u32x4*)(wl2 + 9216);
  AL[3] = *(LAS u32x4*)(wl3 + 24576);
  AL[4] = *(LAS u32x4*)(wl3 + 25600);
  STAMPH(23);
  asm volatile("s_waitcnt vmcnt(11)" : "+v"(H[2][0]) :: "memory");
  o[0][0] = mfma16(AL[0], H[2][0], o[0][0]);
  o[1][0] = mfma16(AL[1], H[2][0], o[1][0]);
  o[2][0] = mfma16(AL[2], H[2][0], o[2][0]);
  o[3][0] = mfma16(AL[3], H[2][0], o[3][0]);
  o[4][0] = mfma16(AL[4], H[2][0], o[4][0]);
  SCHED0();
  LD128SC1(H[2][0], vl16, rws, hrd[0] + 20480, 0);
  asm volatile("s_waitcnt vmcnt(11)" : "+v"(H[2][1]) :: "memory");
  o[0][1] = mfma16(AL[0], H[2][1], o[0][1]);
  o[1][1] = mfma16(AL[1], H[2][1], o[1][1]);
  o[2][1] = mfma16(AL[2], H[2][1], o[2][1]);
  o[3][1] = mfma16(AL[3], H[2][1], o[3][1]);
  o[4][1] = mfma16(AL[4], H[2][1], o[4][1]);
  SCHED0();
  LD128SC1(H[2][1], vl16, rws, hrd[1] + 20480, 0);
  asm volatile("s_waitcnt vmcnt(11)" : "+v"(H[2][2]) :: "memory");
  o[0][2] = mfma16(AL[0], H[2][2], o[0][2]);
  o[1][2] = mfma16(AL[1], H[2][2], o[1][2]);
  o[2][2] = mfma16(AL[2], H[2][2], o[2][2]);
  o[3][2] = mfma16(AL[3], H[2][2], o[3][2]);
  o[4][2] = mfma16(AL[4], H[2][2], o[4][2]);
  SCHED0();
  LD128SC1(H[2][2], vl16, rws, hrd[2] + 20480, 0);
  asm volatile("s_waitcnt vmcnt(11)" : "+v"(H[2][3]) :: "memory");
  o[0][3] = mfma16(AL[0], H[2][3], o[0][3]);
  o[1][3] = mfma16(AL[1], H[2][3], o[1][3]);
  o[2][3] = mfma16(AL[2], H[2][3], o[2][3]);
  o[3][3] = mfma16(AL[3], H[2][3], o[3][3]);
  o[4][3] = mfma16(AL[4], H[2][3], o[4][3]);
  SCHED0();
  LD128SC1(H[2][3], vl16, rws, hrd[3] + 20480, 0);
  AL[0] = *(LAS u32x4*)(wl3 + 26624);
  AL[1] = *(LAS u32x4*)(wl3 + 36864);
  AL[2] = *(LAS u32x4*)(wl3 + 37888);
  AL[3] = *(LAS u32x4*)(wl3 + 38912);
  AL[4] = W19;
  asm volatile("s_waitcnt vmcnt(11)" : "+v"(H[0][0]) :: "memory");
  o[0][0] = mfma16(AL[0], H[0][0], o[0][0]);
  o[1][0] = mfma16(AL[1], H[0][0], o[1][0]);
  o[2][0] = mfma16(AL[2], H[0][0], o[2][0]);
  o[3][0] = mfma16(AL[3], H[0][0], o[3][0]);
  o[4][0] = mfma16(AL[4], H[0][0], o[4][0]);
  SCHED0();
  LD128SC1(H[0][0], vl16, rws, hrd[0] + 24576, 0);
  asm volatile("s_waitcnt vmcnt(11)" : "+v"(H[0][1]) :: "memory");
  o[0][1] = mfma16(AL[0], H[0][1], o[0][1]);
  o[1][1] = mfma16(AL[1], H[0][1], o[1][1]);
  o[2][1] = mfma16(AL[2], H[0][1], o[2][1]);
  o[3][1] = mfma16(AL[3], H[0][1], o[3][1]);
  o[4][1] = mfma16(AL[4], H[0][1], o[4][1]);
  SCHED0();
  LD128SC1(H[0][1], vl16, rws, hrd[1] + 24576, 0);
  asm volatile("s_waitcnt vmcnt(11)" : "+v"(H[0][2]) :: "memory");
  o[0][2] = mfma16(AL[0], H[0][2], o[0][2]);
  o[1][2] = mfma16(AL[1], H[0][2], o[1][2]);
  o[2][2] = mfma16(AL[2], H[0][2], o[2][2]);
  o[3][2] = mfma16(AL[3], H[0][2], o[3][2]);
  o[4][2] = mfma16(AL[4], H[0][2], o[4][2]);
  SCHED0();
  LD128SC1(H[0][2], vl16, rws, hrd[2] + 24576, 0);
  asm volatile("s_waitcnt vmcnt(11)" : "+v"(H[0][3]) :: "memory");
  o[0][3] = mfma16(AL[0], H[0][3], o[0][3]);
  o[1][3] = mfma16(AL[1], H[0][3], o[1][3]);
  o[2][3] = mfma16(AL[2], H[0][3], o[2][3]);
  o[3][3] = mfma16(AL[3], H[0][3], o[3][3]);
  o[4][3] = mfma16(AL[4], H[0][3], o[4][3]);
  SCHED0();
  LD128SC1(H[0][3], vl16, rws, hrd[3] + 24576, 0);
  STAMPH(24);
  asm volatile("s_waitcnt vmcnt(11)" : "+v"(H[1][0]) :: "memory");
  o[0][0] = mfma16(Wr[0][0], H[1][0], o[0][0]);
  o[1][0] = mfma16(Wr[0][1], H[1][0], o[1][0]);
  o[2][0] = mfma16(Wr[0][2], H[1][0], o[2][0]);
  o[3][0] = mfma16(Wr[0][3], H[1][0], o[3][0]);
  o[4][0] = mfma16(Wr[0][4], H[1][0], o[4][0]);
  SCHED0();
  LD128SC1(H[1][0], vl16, rws, hrd[0] + 28672, 0);
  asm volatile("s_waitcnt vmcnt(11)" : "+v"(H[1][1]) :: "memory");
  o[0][1] = mfma16(Wr[0][0], H[1][1], o[0][1]);
  o[1][1] = mfma16(Wr[0][1], H[1][1], o[1][1]);
  o[2][1] = mfma16(Wr[0][2], H[1][1], o[2][1]);
  o[3][1] = mfma16(Wr[0][3], H[1][1], o[3][1]);
  o[4][1] = mfma16(Wr[0][4], H[1][1], o[4][1]);
  SCHED0();
  LD128SC1(H[1][1], vl16, rws, hrd[1] + 28672, 0);
  asm volatile("s_waitcnt vmcnt(11)" : "+v"(H[1][2]) :: "memory");
  o[0][2] = mfma16(Wr[0][0], H[1][2], o[0][2]);
  o[1][2] = mfma16(Wr[0][1], H[1][2], o[1][2]);
  o[2][2] = mfma16(Wr[0][2], H[1][2], o[2][2]);
  o[3][2] = mfma16(Wr[0][3], H[1][2], o[3][2]);
  o[4][2] = mfma16(Wr[0][4], H[1][2], o[4][2]);
  SCHED0();
  LD128SC1(H[1][2], vl16, rws, hrd[2] + 28672, 0);
  asm volatile("s_waitcnt vmcnt(11)" : "+v"(H[1][3]) :: "memory");
  o[0][3] = mfma16(Wr[0][0], H[1][3], o[0][3]);
  o[1][3] = mfma16(Wr[0][1], H[1][3], o[1][3]);
  o[2][3] = mfma16(Wr[0][2], H[1][3], o[2][3]);
  o[3][3] = mfma16(Wr[0][3], H[1][3], o[3][3]);
  o[4][3] = mfma16(Wr[0][4], H[1][3], o[4][3]);
  SCHED0();
  LD128SC1(H[1][3], vl16, rws, hrd[3] + 28672, 0);
  asm volatile("s_waitcnt vmcnt(11)" : "+v"(H[2][0]) :: "memory");
  o[0][0] = mfma16(Wr[1][0], H[2][0], o[0][0]);
  o[1][0] = mfma16(Wr[1][1], H[2][0], o[1][0]);
  o[2][0] = mfma16(Wr[1][2], H[2][0], o[2][0]);
  o[3][0] = mfma16(Wr[1][3], H[2][0], o[3][0]);
  o[4][0] = mfma16(Wr[1][4], H[2][0], o[4][0]);
  SCHED0();
  LD128SC1(H[2][0], vl16, rws, hrd[0] + 32768, 0);
  LD128SC1(Wr[0][0], vl16, rws, w2base + 40960, 0);
  LD128SC1(Wr[0][1], vl16, rws, w2base + 40960, 1024);
  asm volatile("s_waitcnt vmcnt(13)" : "+v"(H[2][1]) :: "memory");
  o[0][1] = mfma16(Wr[1][0], H[2][1], o[0][1]);
  o[1][1] = mfma16(Wr[1][1], H[2][1], o[1][1]);
  o[2][1] = mfma16(Wr[1][2], H[2][1], o[2][1]);
  o[3][1] = mfma16(Wr[1][3], H[2][1], o[3][1]);
  o[4][1] = mfma16(Wr[1][4], H[2][1], o[4][1]);
  SCHED0();
  LD128SC1(H[2][1], vl16, rws, hrd[1] + 32768, 0);
  LD128SC1(Wr[0][2], vl16, rws, w2base + 40960, 2048);
  asm volatile("s_waitcnt vmcnt(14)" : "+v"(H[2][2]) :: "memory");
  o[0][2] = mfma16(Wr[1][0], H[2][2], o[0][2]);
  o[1][2] = mfma16(Wr[1][1], H[2][2], o[1][2]);
  o[2][2] = mfma16(Wr[1][2], H[2][2], o[2][2]);
  o[3][2] = mfma16(Wr[1][3], H[2][2], o[3][2]);
  o[4][2] = mfma16(Wr[1][4], H[2][2], o[4][2]);
  SCHED0();
  LD128SC1(H[2][2], vl16, rws, hrd[2] + 32768, 0);
  LD128SC1(Wr[0][3], vl16, rws, w2base + 40960, 3072);
  asm volatile("s_waitcnt vmcnt(15)" : "+v"(H[2][3]) :: "memory");
  o[0][3] = mfma16(Wr[1][0], H[2][3], o[0][3]);
  o[1][3] = mfma16(Wr[1][1], H[2][3], o[1][3]);
  o[2][3] = mfma16(Wr[1][2], H[2][3], o[2][3]);
  o[3][3] = mfma16(Wr[1][3], H[2][3], o[3][3]);
  o[4][3] = mfma16(Wr[1][4], H[2][3], o[4][3]);
  SCHED0();
  LD128SC1(H[2][3], vl16, rws, hrd[3] + 32768, 0);
  LD128SC1(Wr[0][4], vl16, rws, w2base + 45056, 0);
  STAMPH(25);
  asm volatile("s_waitcnt vmcnt(16)" : "+v"(H[0][0]) :: "memory");
  o[0][0] = mfma16(Wr[2][0], H[0][0], o[0][0]);
  o[1][0] = mfma16(Wr[2][1], H[0][0], o[1][0]);
  o[2][0] = mfma16(Wr[2][2], H[0][0], o[2][0]);
  o[3][0] = mfma16(Wr[2][3], H[0][0], o[3][0]);
  o[4][0] = mfma16(Wr[2][4], H[0][0], o[4][0]);
  SCHED0();
  LD128SC1(H[0][0], vl16, rws, hrd[0] + 36864, 0);
  LD128SC1(Wr[1][0], vl16, rws, w2base + 46080, 0);
  LD128SC1(Wr[1][1], vl16, rws, w2base + 46080, 1024);
  asm volatile("s_waitcnt vmcnt(18)" : "+v"(H[0][1]) :: "memory");
  o[0][1] = mfma16(Wr[2][0], H[0][1], o[0][1]);
  o[1][1] = mfma16(Wr[2][1], H[0][1], o[1][1]);
  o[2][1] = mfma16(Wr[2][2], H[0][1], o[2][1]);
  o[3][1] = mfma16(Wr[2][3], H[0][1], o[3][1]);
  o[4][1] = mfma16(Wr[2][4], H[0][1], o[4][1]);
  SCHED0();
  LD128SC1(H[0][1], vl16, rws, hrd[1] + 36864, 0);
  LD128SC1(Wr[1][2], vl16, rws, w2base + 46080, 2048);
  asm volatile("s_waitcnt vmcnt(19)" : "+v"(H[0][2]) :: "memory");
  o[0][2] = mfma16(Wr[2][0], H[0][2], o[0][2]);
  o[1][2] = mfma16(Wr[2][1], H[0][2], o[1][2]);
  o[2][2] = mfma16(Wr[2][2], H[0][2], o[2][2]);
  o[3][2] = mfma16(Wr[2][3], H[0][2], o[3][2]);
  o[4][2] = mfma16(Wr[2][4], H[0][2], o[4][2]);
  SCHED0();
  LD128SC1(H[0][2], vl16, rws, hrd[2] + 36864, 0);
  LD128SC1(Wr[1][3], vl16, rws, w2base + 46080, 3072);
  asm volatile("s_waitcnt vmcnt(20)" : "+v"(H[0][3]) :: "memory");
  o[0][3] = mfma16(Wr[2][0], H[0][3], o[0][3]);
  o[1][3] = mfma16(Wr[2][1], H[0][3], o[1][3]);
  o[2][3] = mfma16(Wr[2][2], H[0][3], o[2][3]);
  o[3][3] = mfma16(Wr[2][3], H[0][3], o[3][3]);
  o[4][3] = mfma16(Wr[2][4], H[0][3], o[4][3]);
  SCHED0();
  LD128SC1(H[0][3], vl16, rws, hrd[3] + 36864, 0);
  LD128SC1(Wr[1][4], vl16, rws, w2base + 50176, 0);
  asm volatile("s_waitcnt vmcnt(21)" : "+v"(H[1][0]) :: "memory");
  o[0][0] = mfma16(Wr[3][0], H[1][0], o[0][0]);
  o[1][0] = mfma16(Wr[3][1], H[1][0], o[1][0]);
  o[2][0] = mfma16(Wr[3][2], H[1][0], o[2][0]);
  o[3][0] = mfma16(Wr[3][3], H[1][0], o[3][0]);
  o[4][0] = mfma16(Wr[3][4], H[1][0], o[4][0]);
  SCHED0();
  LD128SC1(H[1][0], vl16, rws, hrd[0] + 40960, 0);
  LD128SC1(Wr[2][0], vl16, rws, w2base + 51200, 0);
  LD128SC1(Wr[2][1], vl16, rws, w2base + 51200, 1024);
  asm volatile("s_waitcnt vmcnt(23)" : "+v"(H[1][1]) :: "memory");
  o[0][1] = mfma16(Wr[3][0], H[1][1], o[0][1]);
  o[1][1] = mfma16(Wr[3][1], H[1][1], o[1][1]);
  o[2][1] = mfma16(Wr[3][2], H[1][1], o[2][1]);
  o[3][1] = mfma16(Wr[3][3], H[1][1], o[3][1]);
  o[4][1] = mfma16(Wr[3][4], H[1][1], o[4][1]);
  SCHED0();
  LD128SC1(H[1][1], vl16, rws, hrd[1] + 40960, 0);
  LD128SC1(Wr[2][2], vl16, rws, w2base + 51200, 2048);
  asm volatile("s_waitcnt vmcnt(24)" : "+v"(H[1][2]) :: "memory");
  o[0][2] = mfma16(Wr[3][0], H[1][2], o[0][2]);
  o[1][2] = mfma16(Wr[3][1], H[1][2], o[1][2]);
  o[2][2] = mfma16(Wr[3][2], H[1][2], o[2][2]);
  o[3][2] = mfma16(Wr[3][3], H[1][2], o[3][2]);
  o[4][2] = mfma16(Wr[3][4], H[1][2], o[4][2]);
  SCHED0();
  LD128SC1(H[1][2], vl16, rws, hrd[2] + 40960, 0);
  LD128SC1(Wr[2][3], vl16, rws, w2base + 51200, 3072);
  asm volatile("s_waitcnt vmcnt(25)" : "+v"(H[1][3]) :: "memory");
  o[0][3] = mfma16(Wr[3][0], H[1][3], o[0][3]);
  o[1][3] = mfma16(Wr[3][1], H[1][3], o[1][3]);
  o[2][3] = mfma16(Wr[3][2], H[1][3], o[2][3]);
  o[3][3] = mfma16(Wr[3][3], H[1][3], o[3][3]);
  o[4][3] = mfma16(Wr[3][4], H[1][3], o[4][3]);
  SCHED0();
  LD128SC1(H[1][3], vl16, rws, hrd[3] + 40960, 0);
  LD128SC1(Wr[2][4], vl16, rws, w2base + 55296, 0);
  STAMPH(26);
  asm volatile("s_waitcnt vmcnt(18)" : "+v"(H[2][0]), "+v"(Wr[0][0]), "+v"(Wr[0][1]), "+v"(Wr[0][2]), "+v"(Wr[0][3]), "+v"(Wr[0][4]) :: "memory");
  o[0][0] = mfma16(Wr[0][0], H[2][0], o[0][0]);
  o[1][0] = mfma16(Wr[0][1], H[2][0], o[1][0]);
  o[2][0] = mfma16(Wr[0][2], H[2][0], o[2][0]);
  o[3][0] = mfma16(Wr[0][3], H[2][0], o[3][0]);
  o[4][0] = mfma16(Wr[0][4], H[2][0], o[4][0]);
  SCHED0();
  LD128SC1(H[2][0], vl16, rws, hrd[0] + 45056, 0);
  LD128SC1(Wr[3][0], vl16, rws, w2base + 56320, 0);
  LD128SC1(Wr[3][1], vl16, rws, w2base + 56320, 1024);
  asm volatile("s_waitcnt vmcnt(26)" : "+v"(H[2][1]) :: "memory");
  o[0][1] = mfma16(Wr[0][0], H[2][1], o[0][1]);
  o[1][1] = mfma16(Wr[0][1], H[2][1], o[1][1]);
  o[2][1] = mfma16(Wr[0][2], H[2][1], o[2][1]);
  o[3][1] = mfma16(Wr[0][3], H[2][1], o[3][1]);
  o[4][1] = mfma16(Wr[0][4], H[2][1], o[4][1]);
  SCHED0();
  LD128SC1(H[2][1], vl16, rws, hrd[1] + 45056, 0);
  LD128SC1(Wr[3][2], vl16, rws, w2base + 56320, 2048);
  asm volatile("s_waitcnt vmcnt(26)" : "+v"(H[2][2]) :: "memory");
  o[0][2] = mfma16(Wr[0][0], H[2][2], o[0][2]);
  o[1][2] = mfma16(Wr[0][1], H[2][2], o[1][2]);
  o[2][2] = mfma16(Wr[0][2], H[2][2], o[2][2]);
  o[3][2] = mfma16(Wr[0][3], H[2][2], o[3][2]);
  o[4][2] = mfma16(Wr[0][4], H[2][2], o[4][2]);
  SCHED0();
  LD128SC1(H[2][2], vl16, rws, hrd[2] + 45056, 0);
  LD128SC1(Wr[3][3], vl16, rws, w2base + 56320, 3072);
  asm volatile("s_waitcnt vmcnt(26)" : "+v"(H[2][3]) :: "memory");
  o[0][3] = mfma16(Wr[0][0], H[2][3], o[0][3]);
  o[1][3] = mfma16(Wr[0][1], H[2][3], o[1][3]);
  o[2][3] = mfma16(Wr[0][2], H[2][3], o[2][3]);
  o[3][3] = mfma16(Wr[0][3], H[2][3], o[3][3]);
  o[4][3] = mfma16(Wr[0][4], H[2][3], o[4][3]);
  SCHED0();
  LD128SC1(H[2][3], vl16, rws, hrd[3] + 45056, 0);
  LD128SC1(Wr[3][4], vl16, rws, w2base + 60416, 0);
  asm volatile("s_waitcnt vmcnt(18)" : "+v"(H[0][0]), "+v"(Wr[1][0]), "+v"(Wr[1][1]), "+v"(Wr[1][2]), "+v"(Wr[1][3]), "+v"(Wr[1][4]) :: "memory");
  o[0][0] = mfma16(Wr[1][0], H[0][0], o[0][0]);
  o[1][0] = mfma16(Wr[1][1], H[0][0], o[1][0]);
  o[2][0] = mfma16(Wr[1][2], H[0][0], o[2][0]);
  o[3][0] = mfma16(Wr[1][3], H[0][0], o[3][0]);
  o[4][0] = mfma16(Wr[1][4], H[0][0], o[4][0]);
  SCHED0();
  LD32SC1(sfv, vsf, rws, 0);
  asm volatile("s_waitcnt vmcnt(24)" : "+v"(H[0][1]) :: "memory");
  o[0][1] = mfma16(Wr[1][0], H[0][1], o[0][1]);
  o[1][1] = mfma16(Wr[1][1], H[0][1], o[1][1]);
  o[2][1] = mfma16(Wr[1][2], H[0][1], o[2][1]);
  o[3][1] = mfma16(Wr[1][3], H[0][1], o[3][1]);
  o[4][1] = mfma16(Wr[1][4], H[0][1], o[4][1]);
  SCHED0();
  LD64(xr[0], vxo, rx, 0, 0);
  LD64(xr[1], vxo, rx, 0, 32);
  LD64(xr[2], vxo, rx, 0, 64);
  asm volatile("s_waitcnt vmcnt(25)" : "+v"(H[0][2]) :: "memory");
  o[0][2] = mfma16(Wr[1][0], H[0][2], o[0][2]);
  o[1][2] = mfma16(Wr[1][1], H[0][2], o[1][2]);
  o[2][2] = mfma16(Wr[1][2], H[0][2], o[2][2]);
  o[3][2] = mfma16(Wr[1][3], H[0][2], o[3][2]);
  o[4][2] = mfma16(Wr[1][4], H[0][2], o[4][2]);
  SCHED0();
  LD64(xr[3], vxo, rx, 0, 96);
  LD64(xr[4], vxo, rx, 0, 128);
  asm volatile("s_waitcnt vmcnt(25)" : "+v"(H[0][3]) :: "memory");
  o[0][3] = mfma16(Wr[1][0], H[0][3], o[0][3]);
  o[1][3] = mfma16(Wr[1][1], H[0][3], o[1][3]);
  o[2][3] = mfma16(Wr[1][2], H[0][3], o[2][3]);
  o[3][3] = mfma16(Wr[1][3], H[0][3], o[3][3]);
  o[4][3] = mfma16(Wr[1][4], H[0][3], o[4][3]);
  SCHED0();
  LD64(b2r[0], vb2, rb2, 0, 0);
  LD64(b2r[1], vb2, rb2, 0, 32);
  LD64(b2r[2], vb2, rb2, 0, 64);
  LD64(b2r[3], vb2, rb2, 0, 96);
  LD64(b2r[4], vb2, rb2, 0, 128);
  STAMPH(27);
  asm volatile("s_waitcnt vmcnt(20)" : "+v"(H[1][0]), "+v"(Wr[2][0]), "+v"(Wr[2][1]), "+v"(Wr[2][2]), "+v"(Wr[2][3]), "+v"(Wr[2][4]) :: "memory");
  o[0][0] = mfma16(Wr[2][0], H[1][0], o[0][0]);
  o[1][0] = mfma16(Wr[2][1], H[1][0], o[1][0]);
  o[2][0] = mfma16(Wr[2][2], H[1][0], o[2][0]);
  o[3][0] = mfma16(Wr[2][3], H[1][0], o[3][0]);
  o[4][0] = mfma16(Wr[2][4], H[1][0], o[4][0]);
  SCHED0();
  asm volatile("s_waitcnt vmcnt(25)" : "+v"(H[1][1]) :: "memory");
  o[0][1] = mfma16(Wr[2][0], H[1][1], o[0][1]);
  o[1][1] = mfma16(Wr[2][1], H[1][1], o[1][1]);
  o[2][1] = mfma16(Wr[2][2], H[1][1], o[2][1]);
  o[3][1] = mfma16(Wr[2][3], H[1][1], o[3][1]);
  o[4][1] = mfma16(Wr[2][4], H[1][1], o[4][1]);
  SCHED0();
  asm volatile("s_waitcnt vmcnt(23)" : "+v"(H[1][2]) :: "memory");
  o[0][2] = mfma16(Wr[2][0], H[1][2], o[0][2]);
  o[1][2] = mfma16(Wr[2][1], H[1][2], o[1][2]);
  o[2][2] = mfma16(Wr[2][2], H[1][2], o[2][2]);
  o[3][2] = mfma16(Wr[2][3], H[1][2], o[3][2]);
  o[4][2] = mfma16(Wr[2][4], H[1][2], o[4][2]);
  SCHED0();
  asm volatile("s_waitcnt vmcnt(21)" : "+v"(H[1][3]) :: "memory");
  o[0][3] = mfma16(Wr[2][0], H[1][3], o[0][3]);
  o[1][3] = mfma16(Wr[2][1], H[1][3], o[1][3]);
  o[2][3] = mfma16(Wr[2][2], H[1][3], o[2][3]);
  o[3][3] = mfma16(Wr[2][3], H[1][3], o[3][3]);
  o[4][3] = mfma16(Wr[2][4], H[1][3], o[4][3]);
  SCHED0();
  asm volatile("s_waitcnt vmcnt(10)" : "+v"(sfv) :: "memory");
  STAMPR(8);
  if (!__all(sfv == tag)) poll_flag(vsf, rws, tag);
  STAMPR(9);
  LD64SC1(sr[0], vsp, rws, 0, 0);
  LD64SC1(sr[1], vsp, rws, 65536, 0);
  LD64SC1(sr[2], vsp, rws, 131072, 0);
  LD64SC1(sr[3], vsp, rws, 196608, 0);
  LD64SC1(sr[4], vsp, rws, 262144, 0);
  asm volatile("s_waitcnt vmcnt(16)" : "+v"(H[2][0]), "+v"(Wr[3][0]), "+v"(Wr[3][1]), "+v"(Wr[3][2]), "+v"(Wr[3][3]), "+v"(Wr[3][4]) :: "memory");
  o[0][0] = mfma16(Wr[3][0], H[2][0], o[0][0]);
  o[1][0] = mfma16(Wr[3][1], H[2][0], o[1][0]);
  o[2][0] = mfma16(Wr[3][2], H[2][0], o[2][0]);
  o[3][0] = mfma16(Wr[3][3], H[2][0], o[3][0]);
  o[4][0] = mfma16(Wr[3][4], H[2][0], o[4][0]);
  SCHED0();
  asm volatile("s_waitcnt vmcnt(21)" : "+v"(H[2][1]) :: "memory");
  o[0][1] = mfma16(Wr[3][0], H[2][1], o[0][1]);
  o[1][1] = mfma16(Wr[3][1], H[2][1], o[1][1]);
  o[2][1] = mfma16(Wr[3][2], H[2][1], o[2][1]);
  o[3][1] = mfma16(Wr[3][3], H[2][1], o[3][1]);
  o[4][1] = mfma16(Wr[3][4], H[2][1], o[4][1]);
  SCHED0();
  asm volatile("s_waitcnt vmcnt(19)" : "+v"(H[2][2]) :: "memory");
  o[0][2] = mfma16(Wr[3][0], H[2][2], o[0][2]);
  o[1][2] = mfma16(Wr[3][1], H[2][2], o[1][2]);
  o[2][2] = mfma16(Wr[3][2], H[2][2], o[2][2]);
  o[3][2] = mfma16(Wr[3][3], H[2][2], o[3][2]);
  o[4][2] = mfma16(Wr[3][4], H[2][2], o[4][2]);
  SCHED0();
  asm volatile("s_waitcnt vmcnt(17)" : "+v"(H[2][3]) :: "memory");
  o[0][3] = mfma16(Wr[3][0], H[2][3], o[0][3]);
  o[1][3] = mfma16(Wr[3][1], H[2][3], o[1][3]);
  o[2][3] = mfma16(Wr[3][2], H[2][3], o[2][3]);
  o[3][3] = mfma16(Wr[3][3], H[2][3], o[3][3]);
  o[4][3] = mfma16(Wr[3][4], H[2][3], o[4][3]);
  SCHED0();
  *(LAS f32x4*)(wl2) = o[0][1]; *(LAS f32x4*)(wl2 + 1024) = o[1][1];
  *(LAS f32x4*)(wl3) = o[2][1]; *(LAS f32x4*)(wl3 + 1024) = o[3][1]; *(LAS f32x4*)(wl3 + 2048) = o[4][1];
  *(LAS f32x4*)(wl2 + 8192) = o[0][2]; *(LAS f32x4*)(wl2 + 9216) = o[1][2];
  *(LAS f32x4*)(wl3 + 24576) = o[2][2]; *(LAS f32x4*)(wl3 + 25600) = o[3][2]; *(LAS f32x4*)(wl3 + 26624) = o[4][2];
  *(LAS f32x4*)(wl3 + 49152) = o[0][3]; *(LAS f32x4*)(wl3 + 50176) = o[1][3]; *(LAS f32x4*)(wl3 + 51200) = o[2][3];
  *(LAS f32x4*)(wl3 + 12288) = o[3][3]; *(LAS f32x4*)(wl3 + 13312) = o[4][3];
  WAITLGKM0B();
  BAR();
  f32x4 res[5];
  {
    LAS char* a2 = L + ((w - 1) & 3) * 2048 + lane * 16;
    LAS char* a3 = L + 16384 + ((w - 1) & 3) * 3072 + lane * 16;
    LAS char* c2 = L + ((w - 2) & 3) * 2048 + lane * 16;
    LAS char* c3 = L + 16384 + ((w - 2) & 3) * 3072 + lane * 16;
    LAS char* e3 = L + 16384 + ((w - 3) & 3) * 3072 + lane * 16;
    res[0] = (o[0][0] + *(LAS f32x4*)(a2)) + (*(LAS f32x4*)(c2 + 8192) + *(LAS f32x4*)(e3 + 49152));
    res[1] = (o[1][0] + *(LAS f32x4*)(a2 + 1024)) + (*(LAS f32x4*)(c2 + 9216) + *(LAS f32x4*)(e3 + 50176));
    res[2] = (o[2][0] + *(LAS f32x4*)(a3)) + (*(LAS f32x4*)(c3 + 24576) + *(LAS f32x4*)(e3 + 51200));
    res[3] = (o[3][0] + *(LAS f32x4*)(a3 + 1024)) + (*(LAS f32x4*)(c3 + 25600) + *(LAS f32x4*)(e3 + 12288));
    res[4] = (o[4][0] + *(LAS f32x4*)(a3 + 2048)) + (*(LAS f32x4*)(c3 + 26624) + *(LAS f32x4*)(e3 + 13312));
  }
  asm volatile("s_waitcnt vmcnt(0)"
               : "+v"(xr[0]), "+v"(xr[1]), "+v"(xr[2]), "+v"(xr[3]), "+v"(xr[4]), "+v"(b2r[0]), "+v"(b2r[1]), "+v"(b2r[2]), "+v"(b2r[3]),
                 "+v"(b2r[4]), "+v"(sr[0]), "+v"(sr[1]), "+v"(sr[2]), "+v"(sr[3]), "+v"(sr[4])::"memory");
  STAMPR(10);
#pragma unroll
  for (int nf = 0; nf < 5; ++nf) {
    u32x2 ov;
#pragma unroll
    for (int e = 0; e < 2; ++e) {
      f32x2 v = {res[nf][2 * e] + bflo(b2r[nf][e]), res[nf][2 * e + 1] + bfhi(b2r[nf][e])};
      f32x2 dd = {__builtin_amdgcn_exp2f(v.x * -1.4426950408889634f), __builtin_amdgcn_exp2f(v.y * -1.4426950408889634f)};
      dd += 1.f;
      f32x2 gg = {v.x * __builtin_amdgcn_rcpf(dd.x), v.y * __builtin_amdgcn_rcpf(dd.y)};
      f32x2 ss = {bflo(sr[nf][e]), bfhi(sr[nf][e])}, xx = {bflo(xr[nf][e]), bfhi(xr[nf][e])};
      f32x2 t = ss * gg + xx;
      ov[e] = pkbf(t.x, t.y);
    }
    __builtin_amdgcn_raw_buffer_store_b64(ov, rout, vxo + nf * 32, 0, 0);
  }
  STAMPR(11);
}

__device__ __forceinline__ void kgen_pass(float are, float aim, float bre, float bim, float cre, float cim, float dt, LAS char* T, int lane,
                                          f32x16& kacc) {
  const float a2 = dt * are * 1.4426950408889634f;
  const float br = dt * aim * 0.15915494309189535f;
  f32x2 g1, G;
  {
    const float e1 = __builtin_amdgcn_exp2f(a2), f1 = __builtin_amdgcn_fractf(br);
    g1 = (f32x2){e1 * __builtin_amdgcn_cosf(f1), e1 * __builtin_amdgcn_sinf(f1)};
    const float e2 = __builtin_amdgcn_exp2f(a2 * 256.f), f2 = __builtin_amdgcn_fractf(br * 256.f);
    G = (f32x2){e2 * __builtin_amdgcn_cosf(f2), e2 * __builtin_amdgcn_sinf(f2)};
  }
  f32x2 W = cmul((f32x2){cre, cim}, (f32x2){dt * bre, dt * bim});
  W.y = -W.y;
  LAS char* tr = T + lane * 112;
  LAS char* tim = tr + 64 * 112;
  const f32x2 g2 = cmul(g1, g1), g3 = cmul(g2, g1), g4 = cmul(g2, g2), g5 = cmul(g4, g1), g6 = cmul(g4, g2), g7 = cmul(g4, g3);
  f32x2 q = cmul(g4, g4);
  q.y = -q.y;
  *(LAS u32x4*)(tr) = (u32x4){pkbf(1.f, g1.x), pkbf(g2.x, g3.x), pkbf(g4.x, g5.x), pkbf(g6.x, g7.x)};
  *(LAS u32x4*)(tim) = (u32x4){pkbf(0.f, g1.y), pkbf(g2.y, g3.y), pkbf(g4.y, g5.y), pkbf(g6.y, g7.y)};
  const f32x2 r9 = cmul(G, g2), r10 = cmul(G, g4), r11 = cmul(G, g6), G2 = cmul(G, G), G3 = cmul(G2, G), r13 = cmul(G2, g4), r15 = cmul(G3, g4);
  *(LAS u32x4*)(tr + 16) = (u32x4){pkbf(G.x, r9.x), pkbf(r10.x, r11.x), pkbf(G2.x, r13.x), pkbf(G3.x, r15.x)};
  *(LAS u32x4*)(tim + 16) = (u32x4){pkbf(G.y, r9.y), pkbf(r10.y, r11.y), pkbf(G2.y, r13.y), pkbf(G3.y, r15.y)};
  const f32x2 G4 = cmul(G2, G2), G5 = cmul(G4, G), G6 = cmul(G4, G2), G7 = cmul(G4, G3), G8 = cmul(G4, G4);
  *(LAS u32x4*)(tr + 32) = (u32x4){pkbf(G4.x, G5.x), pkbf(G6.x, G7.x), pkbf(G8.x, 0.f), 0u};
  *(LAS u32x4*)(tim + 32) = (u32x4){pkbf(G4.y, G5.y), pkbf(G6.y, G7.y), pkbf(G8.y, 0.f), 0u};
  const f32x2 q2 = cmul(q, q), q4 = cmul(q2, q2);
  f32x2 z0 = W, z1 = cmul(W, q), z2 = cmul(W, q2), z3 = cmul(z1, q2);
#pragma unroll
  for (int c = 0; c < 4; ++c) {
    const f32x2 y0 = cmul(z0, q4), y1 = cmul(z1, q4), y2 = cmul(z2, q4), y3 = cmul(z3, q4);
    *(LAS u32x4*)(tr + 48 + 16 * c) = (u32x4){pkbf(z0.x, z1.x), pkbf(z2.x, z3.x), pkbf(y0.x, y1.x), pkbf(y2.x, y3.x)};
    *(LAS u32x4*)(tim + 48 + 16 * c) = (u32x4){pkbf(z0.y, z1.y), pkbf(z2.y, z3.y), pkbf(y0.y, y1.y), pkbf(y2.y, y3.y)};
    if (c < 3) { z0 = cmul(y0, q4); z1 = cmul(y1, q4); z2 = cmul(y2, q4); z3 = cmul(y3, q4); }
  }
  const int gm = lane >> 4;
  LAS char* ra = T + (8 * (gm >> 1) + ((lane >> 2) & 3)) * 112 + 32 * (gm & 1) + 8 * (lane & 3);
#pragma unroll
  for (int ks = 0; ks < 8; ++ks) {
    const u32x4 A = trrd2(ra + ks * 1792, ra + ks * 1792 + 448);
    const u32x4 B = trrd2(ra + ks * 1792 + 48, ra + ks * 1792 + 448 + 48);
    kacc = mfma32(A, B, kacc);
  }
}

constexpr int U_STRIDE = 8336, U_ODD = 4160, KP_OFF = 57344, KP_STRIDE = 4576;

__device__ __forceinline__ void ln_rows(const u32x2* xv, int tb, int w, int lane, u32 tag, rsrc_t rws, LAS char* L) {
  const int hl = lane >> 5, li = lane & 31, rr = 2 * w + hl;
  float v[20];
  float s = 0.f;
#pragma unroll
  for (int k = 0; k < 5; ++k) {
    v[4 * k] = bflo(xv[k].x); v[4 * k + 1] = bfhi(xv[k].x); v[4 * k + 2] = bflo(xv[k].y); v[4 * k + 3] = bfhi(xv[k].y);
    s += (v[4 * k] + v[4 * k + 1]) + (v[4 * k + 2] + v[4 * k + 3]);
  }
  s = red32(s);
  const float mean = s * (1.f / 640.f);
  float qq = 0.f;
#pragma unroll
  for (int k = 0; k < 20; ++k) { const float dd = v[k] - mean; qq += dd * dd; }
  qq = red32(qq);
  const float var = rbf(qq * (1.f / 640.f)), mb = rbf(mean);
  const float den = rbf(sqrtf(rbf(var + 1e-5f)));
  const float rden = __builtin_amdgcn_rcpf(den);
  if (li == 0) __builtin_amdgcn_raw_buffer_store_b64((u32x2){pkbf(mb, den), tag}, rws, WS_ST + (8 * tb + rr) * 8, 0, 16);
  LAS char* st = L + KP_OFF + rr * 1344 + li * 8;
#pragma unroll
  for (int k = 0; k < 5; ++k) {
    const u32 t0 = pkbf(v[4 * k] - mb, v[4 * k + 1] - mb), t1 = pkbf(v[4 * k + 2] - mb, v[4 * k + 3] - mb);
    *(LAS u32x2*)(st + k * 256) = (u32x2){pkbf(bflo(t0) * rden, bfhi(t0) * rden), pkbf(bflo(t1) * rden, bfhi(t1) * rden)};
  }
  WAITLGKM0B();
  BAR();
  LAS char* rd = L + KP_OFF + ((lane >> 2) & 3) * 1344 + (lane >> 4) * 32 + (lane & 3) * 8;
#pragma unroll 1
  for (int it = w; it < 10; it += 4) {
    const u32x4 t = trrd2(rd + it * 128, rd + it * 128 + 4 * 1344);
    __builtin_amdgcn_raw_buffer_store_b128(t, rws, WS_XN + (tb * 640 + it * 64 + lane) * 16, 0, 16);
  }
}

__device__ __forceinline__ void retile_role(const u16* x, const u16* w2, char* ws, LAS char* L, int tb, int jb) {
  const int tid = threadIdx.x, lane = tid & 63;
  const int w = __builtin_amdgcn_readfirstlane(tid >> 6);
  STAMPS_DECL
  STAMP(0);
  const rsrc_t rx = mkrs(x, 2048 * 640 * 2), rws = mkrs(ws, WS_SPAN), rw2 = mkrs(w2, 1536 * 640 * 2);
  u32x4 wv[5];
  u32x2 xv[5];
  u32 lo[5];
#pragma unroll
  for (int it = 0; it < 5; ++it) {
    const int id = it * 256 + tid, rw = id / 40, cc = id % 40;
    wv[it] = __builtin_amdgcn_raw_buffer_load_b128(rw2, (32 * (jb >> 1) + rw) * 1280 + (jb & 1) * 640 + cc * 16, 0, 0);
    lo[it] = rw * 672 + cc * 16;
  }
#pragma unroll
  for (int k = 0; k < 5; ++k) xv[k] = __builtin_amdgcn_raw_buffer_load_b64(rx, (8 * tb + 2 * w + (lane >> 5)) * 1280 + k * 256 + (lane & 31) * 8, 0, 0);
  const u32 tag = *(cptr32)(uintptr_t)ws + TAG_ADD;
#pragma unroll
  for (int it = 0; it < 5; ++it) *(LAS u32x4*)(L + lo[it]) = wv[it];
  WAITLGKM0B();
  BAR();
  LAS char* rd = L + (4 * (lane >> 4) + ((lane >> 2) & 3)) * 672 + 160 * w + 8 * (lane & 3);
#pragma unroll
  for (int nf = 0; nf < 5; ++nf) {
    const u32x4 t = trrd2(rd + 32 * nf, rd + 32 * nf + 16 * 672);
    __builtin_amdgcn_raw_buffer_store_b128(t, rws, WS_W2P + (((4 * (jb & 1) + w) * 48 + (jb >> 1)) * 5 + nf) * 1024 + lane * 16, 0, 16);
  }
  STAMP(1);
  ln_rows(xv, tb, w, lane, tag, rws, L);
  STAMP(2);
  WAITVM(0);
  BAR();
  if (tid == 0) {
    __builtin_amdgcn_raw_buffer_store_b32(tag, rws, WS_W2F + jb * 4, 0, 16);
    __builtin_amdgcn_raw_buffer_store_b32(tag, rws, WS_LNF + tb * 4, 0, 16);
  }
  STAMP(3);
  if (jb == 0 && w == 0) {
    _Pragma("unroll 1") for (int sp = 0; sp < SPIN_MAX; ++sp) {
      __builtin_amdgcn_s_sleep(32);
      const u32x4 lf = __builtin_amdgcn_raw_buffer_load_b128(rws, WS_LNF + lane * 16, 0, 16);
      u32 bad = (lf.x ^ tag) | (lf.y ^ tag) | (lf.z ^ tag) | (lf.w ^ tag);
#pragma unroll
      for (int u = 0; u < 4; ++u) bad |= __builtin_amdgcn_raw_buffer_load_b32(rws, WS_HF + (u * 64 + lane) * 128, 0, 16) ^ tag;
      if (__all(bad == 0)) break;
      asm volatile("" ::: "memory");
    }
    if (lane == 0) __builtin_amdgcn_raw_buffer_store_b32(tag, rws, 0, 0, 16);
  }
}

__device__ __forceinline__ void chan_role(const u16* x, const u16* Are, const u16* Aim, const u16* Bre, const u16* Bim, const u16* Cre,
                                          const u16* Cim, const u16* ldt, const u16* dsk, char* ws, LAS char* L, int tb, int G) {
  const int tid = threadIdx.x, lane = tid & 63;
  const int w = __builtin_amdgcn_readfirstlane(tid >> 6);
  STAMPS_DECL
  STAMP(0);
  const rsrc_t rx = mkrs(x, 2048 * 640 * 2), rws = mkrs(ws, WS_SPAN);
  const int hl = lane >> 5, li = lane & 31;
  const int d = 4 * G + w;
  u32x2 xv[5];
#pragma unroll
  for (int k = 0; k < 5; ++k) xv[k] = __builtin_amdgcn_raw_buffer_load_b64(rx, (8 * tb + 2 * w + hl) * 1280 + k * 256 + li * 8, 0, 0);
  u32 p01[6], p2[6];
  {
    const u16* pp[6] = {Are, Aim, Bre, Bim, Cre, Cim};
#pragma unroll
    for (int k = 0; k < 6; ++k) p01[k] = *(const GAS u32*)((const GAS char*)pp[k] + d * 384 + lane * 4);
#pragma unroll
    for (int k = 0; k < 6; ++k) p2[k] = *(const GAS u16*)((const GAS char*)pp[k] + d * 384 + 256 + lane * 2);
  }
  const u32 ldw = *(cptr32)((uintptr_t)ldt + (d >> 1) * 4), dsw = *(cptr32)((uintptr_t)dsk + (d >> 1) * 4);
  const u32 tag = *(cptr32)(uintptr_t)ws + TAG_ADD;
  ln_rows(xv, tb, w, lane, tag, rws, L);
  STAMP(1);
  const float dt = rbf(expf((d & 1) ? bfhi(ldw) : bflo(ldw)));
  f32x16 kacc = (f32x16)0.f;
  LAS char* T = L + w * 14336;
  kgen_pass(bflo(p01[0]), bflo(p01[1]), bflo(p01[2]), bflo(p01[3]), bflo(p01[4]), bflo(p01[5]), dt, T, lane, kacc);
  STAMP(8);
  WAITVM(0);
  BAR();
  if (tid == 0) __builtin_amdgcn_raw_buffer_store_b32(tag, rws, WS_LNF + tb * 4, 0, 16);
  STAMP(2);
  kgen_pass(bfhi(p01[0]), bfhi(p01[1]), bfhi(p01[2]), bfhi(p01[3]), bfhi(p01[4]), bfhi(p01[5]), dt, T, lane, kacc);
  kgen_pass(bflo(p2[0]), bflo(p2[1]), bflo(p2[2]), bflo(p2[3]), bflo(p2[4]), bflo(p2[5]), dt, T, lane, kacc);
  STAMP(3);
  poll_flag(WS_LNF + (64 * w + lane) * 4, rws, tag);
  STAMP(4);
  const int ch = lane & 3, tb0 = 64 * w + 4 * (lane >> 2);
  u32x4 uv[4];
#pragma unroll
  for (int it = 0; it < 4; ++it) uv[it] = __builtin_amdgcn_raw_buffer_load_b128(rws, WS_XN + ((tb0 + it) * 640 + 4 * G + ch) * 16, 0, 16);
  LAS char* KZ = L + KP_OFF + w * KP_STRIDE;
  LAS char* K0 = KZ + 480;
  {
    const u32x4 zero4 = {0u, 0u, 0u, 0u};
    if (lane < 30) *(LAS u32x4*)(KZ + lane * 16) = zero4;
    *(LAS u32x2*)(K0 + li * 16 + (hl ? 0 : 8)) = (u32x2){pkbf(kacc[3], kacc[2]), pkbf(kacc[1], kacc[0])};
    const u32 d4 = pkbf(kacc[4], kacc[4]), d5 = pkbf(kacc[5], kacc[5]), d6 = pkbf(kacc[6], kacc[6]), d7 = pkbf(kacc[7], kacc[7]);
    const u32 d8 = pkbf(kacc[8], kacc[8]), d9 = pkbf(kacc[9], kacc[9]), d10 = pkbf(kacc[10], kacc[10]), d11 = pkbf(kacc[11], kacc[11]);
    u32 n4 = shl1(d4), n6 = shl1(d6), n8 = shl1(d8), n9 = shl1(d9), n10 = shl1(d10), n11 = shl1(d11);
    const u32 f4 = __builtin_amdgcn_readlane(d6, 32), f6 = __builtin_amdgcn_readlane(d8, 0), f8 = __builtin_amdgcn_readlane(d9, 0);
    const u32 f9 = __builtin_amdgcn_readlane(d10, 0), f10 = __builtin_amdgcn_readlane(d11, 0), f11 = __builtin_amdgcn_readlane(d8, 32);
    if (lane == 63) { n4 = f4; n6 = f6; }
    if (lane == 31) { n8 = f8; n9 = f9; n10 = f10; n11 = f11; }
    const u32x4 ca = hl ? (u32x4){n4, d5, lohi(d5, d4), d4} : (u32x4){lohi(n4, d7), d6, lohi(d6, d5), d4};
    *(LAS u32x4*)(K0 + ((hl ? 64 : 32) + li) * 16) = ca;
    const bool ev2 = (li & 1) == 0;
    const u32x4 cb = hl ? (u32x4){n6, d7, lohi(d7, d6), d6} : (u32x4){n8, ev2 ? lohi(n8, d8) : n8, d8, d8};
    *(LAS u32x4*)(K0 + ((hl ? 96 : 128) + li) * 16) = cb;
    if (!hl) {
      *(LAS u32x4*)(K0 + (160 + li) * 16) = (u32x4){n9, ev2 ? lohi(n9, d9) : n9, d9, d9};
      *(LAS u32x4*)(K0 + (192 + li) * 16) = (u32x4){n10, ev2 ? lohi(n10, d10) : n10, d10, d10};
      *(LAS u32x4*)(K0 + (224 + li) * 16) = (u32x4){n11, ev2 ? lohi(n11, d11) : n11, d11, d11};
    }
  }
  WAITLGKM0B();
  BAR();
  STAMP(9);
  {
    LAS char* E = L + ch * U_STRIDE + 32 + 16 * tb0;
    LAS char* O = E + U_ODD;
#pragma unroll
    for (int it = 0; it < 4; ++it) *(LAS u32x4*)(E + 16 * it) = uv[it];
    *(LAS u16*)(O + 2) = (u16)uv[0].x;
    *(LAS u32*)(O + 4) = __builtin_amdgcn_alignbit(uv[0].y, uv[0].x, 16);
    *(LAS u32x2*)(O + 8) = (u32x2){__builtin_amdgcn_alignbit(uv[0].z, uv[0].y, 16), __builtin_amdgcn_alignbit(uv[0].w, uv[0].z, 16)};
#pragma unroll
    for (int it = 1; it < 4; ++it)
      *(LAS u32x4*)(O + 16 * it) = (u32x4){__builtin_amdgcn_alignbit(uv[it].x, uv[it - 1].w, 16), __builtin_amdgcn_alignbit(uv[it].y, uv[it].x, 16),
                                           __builtin_amdgcn_alignbit(uv[it].z, uv[it].y, 16), __builtin_amdgcn_alignbit(uv[it].w, uv[it].z, 16)};
    *(LAS u16*)(O + 64) = (u16)(uv[3].w >> 16);
    if (tid < 4) {
      LAS char* Z = L + tid * U_STRIDE;
      const u32x4 zero4 = {0u, 0u, 0u, 0u};
      *(LAS u32x4*)(Z) = zero4; *(LAS u32x4*)(Z + 16) = zero4;
      *(LAS u32x4*)(Z + U_ODD) = zero4; *(LAS u32x4*)(Z + U_ODD + 16) = zero4;
      *(LAS u16*)(Z + U_ODD + 32) = 0;
    }
  }
  WAITLGKM0B();
  BAR();
  STAMP(5);
  f32x4 ya[8];
#pragma unroll
  for (int P = 0; P < 8; ++P) ya[P] = (f32x4)0.f;
  {
    const int q = lane & 15, g = lane >> 4;
    LAS char* ub[4];
#pragma unroll
    for (int k = 0; k < 4; ++k) ub[k] = L + w * U_STRIDE + ((q & 1) ? 2 * (25 + q - 8 * g) : U_ODD + 2 * (26 + q - 8 * g)) + 64 * 6 + 1024 * k;
    LAS char* kb = KZ + 32 * q + 16 * g;
    u32x4 A0[8], B0[8], A1[8], B1[8];
#define LDA(DST, K, OFF)                                                                                                     \
  {                                                                                                                          \
    const u32x2a4 lo_ = *(LAS u32x2a4*)(ub[(K) >> 1] + ((K) & 1) * 512 + (OFF)), hi_ = *(LAS u32x2a4*)(ub[(K) >> 1] + ((K) & 1) * 512 + (OFF) + 8); \
    DST = (u32x4){lo_.x, lo_.y, hi_.x, hi_.y};                                                                               \
  }
#pragma unroll
    for (int k = 0; k < 8; ++k) { B0[k] = *(LAS u32x4*)(kb + 512 * k); LDA(A0[k], k, 64) }
#pragma unroll 1
    for (int cl = 0; cl < 8; cl += 2) {
      SCHED0();
#pragma unroll
      for (int i1 = 0; i1 < 8; ++i1) {
        if (i1 < 4) {
          LDA(A1[2 * i1], 2 * i1, 0) LDA(A1[2 * i1 + 1], 2 * i1 + 1, 0)
          B1[2 * i1] = *(LAS u32x4*)(kb + 64 + 1024 * i1); B1[2 * i1 + 1] = *(LAS u32x4*)(kb + 64 + 1024 * i1 + 512);
        }
#pragma unroll
        for (int j1 = 0; j1 + i1 < 8; ++j1) ya[i1 + j1] = mfma16(A0[j1], B0[i1], ya[i1 + j1]);
        SCHED0();
      }
      kb += (cl < 6) ? 128 : 0;
#pragma unroll
      for (int k = 0; k < 4; ++k) ub[k] -= (cl < 6) ? 128 : 0;
#pragma unroll
      for (int i1 = 0; i1 < 8; ++i1) {
        if (i1 < 4) {
          LDA(A0[2 * i1], 2 * i1, 64) LDA(A0[2 * i1 + 1], 2 * i1 + 1, 64)
          B0[2 * i1] = *(LAS u32x4*)(kb + 1024 * i1); B0[2 * i1 + 1] = *(LAS u32x4*)(kb + 1024 * i1 + 512);
        }
#pragma unroll
        for (int j1 = 0; j1 + i1 < 8; ++j1) ya[i1 + j1] = mfma16(A1[j1], B1[i1], ya[i1 + j1]);
        SCHED0();
      }
    }
  }
  STAMP(6);
  {
    const float dk = (d & 1) ? bfhi(dsw) : bflo(dsw);
    LAS char* e = L + w * U_STRIDE + 32 + 32 * (lane & 15) + 8 * (lane >> 4);
#pragma unroll
    for (int P = 0; P < 8; ++P) {
      const u32x2 u = *(LAS u32x2*)(e + 512 * P);
      const u32 y0 = pkbf(ya[P][0], ya[P][1]), y1 = pkbf(ya[P][2], ya[P][3]);
      const u32 k0 = pkbf(bflo(u.x) * dk, bfhi(u.x) * dk), k1 = pkbf(bflo(u.y) * dk, bfhi(u.y) * dk);
      *(LAS u32x2*)(e + 512 * P) = (u32x2){pkbf(bflo(y0) + bflo(k0), bfhi(y0) + bfhi(k0)), pkbf(bflo(y1) + bflo(k1), bfhi(y1) + bfhi(k1))};
    }
  }
  WAITLGKM0B();
  BAR();
  {
    LAS char* rd = L + ((lane >> 2) & 3) * U_STRIDE + 32 + 2 * (512 * w + 16 * (lane >> 4) + 4 * (lane & 3));
#pragma unroll
    for (int it = 0; it < 8; ++it) {
      const u32x2 t = trrd(rd + 128 * it);
      __builtin_amdgcn_raw_buffer_store_b64(t, rws, WS_SP + G * 16384 + (512 * w + 64 * it + lane) * 8, 0, 16);
    }
  }
  STAMP(10);
  WAITVM(0);
  BAR();
  if (tid == 0) __builtin_amdgcn_raw_buffer_store_b32(tag, rws, WS_SF + 128 * (G / 20) + 4 * (G % 20), 0, 16);
  STAMP(7);
}

__global__ __launch_bounds__(256, 2) void fused_k(const u16* x, const u16* Are, const u16* Aim, const u16* Bre, const u16* Bim, const u16* Cre,
                                                  const u16* Cim, const u16* ldt, const u16* dsk, const u16* w1, const u16* b1, const u16* w2,
                                                  const u16* b2, u16* out, char* ws) {
  __shared__ __attribute__((aligned(16))) char smem[LDS_BYTES];
  LAS char* L = (LAS char*)smem;
  if (blockIdx.x < 256) {
    gemm_role(x, w1, b1, b2, out, ws, L);
  } else {
    const int c = blockIdx.x - 256, xcd = c & 7, r = c >> 3;
    const int tb = 64 * (xcd >> 1) + 32 * (xcd & 1) + r;
    if (r < 20) chan_role(x, Are, Aim, Bre, Bim, Cre, Cim, ldt, dsk, ws, L, tb, 20 * xcd + r);
    else retile_role(x, w2, ws, L, tb, 12 * xcd + r - 20);
  }
}

extern "C" void kernel_launch(void* const* d_in, const int* in_sizes, int n_in, void* d_out, int out_size, void* d_ws, size_t ws_size,
                              hipStream_t stream) {
  fused_k<<<512, 256, 0, stream>>>((const u16*)d_in[0], (const u16*)d_in[1], (const u16*)d_in[2], (const u16*)d_in[3], (const u16*)d_in[4],
                                   (const u16*)d_in[5], (const u16*)d_in[6], (const u16*)d_in[7], (const u16*)d_in[8], (const u16*)d_in[10],
                                   (const u16*)d_in[11], (const u16*)d_in[12], (const u16*)d_in[13], (u16*)d_out, (char*)d_ws);
}
```

```cpp
#include <hip/hip_runtime.h>
#include <stdint.h>

typedef unsigned u32;
typedef unsigned short u16;
typedef unsigned long long u64;
typedef __attribute__((ext_vector_type(8))) __bf16 bf16x8;
typedef __attribute__((ext_vector_type(2))) __bf16 bf16x2;
typedef __attribute__((ext_vector_type(4))) short s16x4;
typedef __attribute__((ext_vector_type(2))) float f32x2;
typedef __attribute__((ext_vector_type(4))) float f32x4;
typedef __attribute__((ext_vector_type(16))) float f32x16;
typedef __attribute__((ext_vector_type(2))) u32 u32x2;
typedef __attribute__((ext_vector_type(4))) u32 u32x4;
typedef __attribute__((ext_vector_type(2), aligned(4))) u32 u32x2a4;
#define LAS __attribute__((address_space(3)))
#define GAS __attribute__((address_space(1)))
typedef __amdgpu_buffer_rsrc_t rsrc_t;

constexpr int D = 640, NM = 192;
constexpr u32 WS_LNF = 4096, WS_SF = 163840, WS_W2F = 131072, WS_HF = 16384, WS_ST = 65536, WS_CS = 98304;
constexpr u32 WS_XN = 1u << 20, WS_SP = 4u << 20, WS_W2P = 7u << 20, WS_HP = 9u << 20, WS_STAMP = 16u << 20, WS_SPAN = 18u << 20;
constexpr u32 TAG_ADD = 0x9E3779B9u;
#define SPINS 400000
constexpr int SPIN_MAX = SPINS;
constexpr int LDS_BYTES = 81920;

#define SSM_FIRST 1
#define CODE_PF 0
#define CODE_LINES 310
#define TLB_PF 0
#define OUT_AUX 0
#define PRIO_G 0
#define PRIO_L 3
#define PRIO_C 0
#define PRIO_C2 2
#define PRIO_C3 2
#define PRIO_G2 3
#define PRIO_G3 0
#define KG_LOOP 0
#define SL_S 4
#define SL_H 1
#define SF_STRIDE 20480
#define W2_PF 0
#define SL_W 8
#define SL_P 32
#define SL_L 2
#define KPK1 1
#define KPK2 1
#define KPK3 1
#define CS_LATE 2
#define GM_H 12
#define PRIO_GM 3
#define EP_LIGHT 1
#define LNF_STR 128
#define LNF_BLK 1
#define HF_BLK 1
#define ST_STR 128
#define XN_EARLY 3
#define PRIO_C1 PRIO_C
#define PF_MODE 2

#define STAMP(k)
#define STAMPC(k)
#define STAMPS_DECL
#define STAMPR(k)
#define STAMPW(k)
#define STAMPH(k)
#define STAMPD(k) STAMPC(k)

__device__ __forceinline__ float bflo(u32 v) { return __uint_as_float(v << 16); }
__device__ __forceinline__ float bfhi(u32 v) { return __uint_as_float(v & 0xffff0000u); }
__device__ __forceinline__ u32 pkbf(float a, float b) {
  f32x2 t = {a, b};
  return __builtin_bit_cast(u32, __builtin_convertvector(t, bf16x2));
}
__device__ __forceinline__ float rbf(float a) { return bflo(pkbf(a, 0.f)); }
__device__ __forceinline__ f32x2 cmul(f32x2 a, f32x2 b) {
  f32x2 t, d;
  asm("v_pk_mul_f32 %0, %1, %2 op_sel:[0,0] op_sel_hi:[0,1]" : "=v"(t) : "v"(a), "v"(b));
  asm("v_pk_fma_f32 %0, %1, %2, %3 op_sel:[1,1,0] op_sel_hi:[1,0,1] neg_lo:[1,0,0]" : "=v"(d) : "v"(a), "v"(b), "v"(t));
  return d;
}
__device__ __forceinline__ f32x4 mfma16(u32x4 a, u32x4 b, f32x4 c) {
  return __builtin_amdgcn_mfma_f32_16x16x32_bf16(__builtin_bit_cast(bf16x8, a), __builtin_bit_cast(bf16x8, b), c, 0, 0, 0);
}
__device__ __forceinline__ f32x16 mfma32(u32x4 a, u32x4 b, f32x16 c) {
  return __builtin_amdgcn_mfma_f32_32x32x16_bf16(__builtin_bit_cast(bf16x8, a), __builtin_bit_cast(bf16x8, b), c, 0, 0, 0);
}
__device__ __forceinline__ u32x2 trrd(LAS char* p) {
  return __builtin_bit_cast(u32x2, __builtin_amdgcn_ds_read_tr16_b64_v4i16((LAS s16x4*)p));
}
__device__ __forceinline__ u32x4 trrd2(LAS char* p0, LAS char* p1) {
  u32x2 a = trrd(p0), b = trrd(p1);
  return (u32x4){a.x, a.y, b.x, b.y};
}
__device__ __forceinline__ rsrc_t mkrs(const void* p, u32 bytes) {
  return __builtin_amdgcn_make_buffer_rsrc((void*)p, (short)0, bytes, 0x00020000);
}
#define WAITVM(n) asm volatile("s_waitcnt vmcnt(" #n ")" ::: "memory")
#define BAR() asm volatile("s_barrier" ::: "memory")
#define WAITLGKM0() asm volatile("s_waitcnt lgkmcnt(0)" ::: "memory")
#define SCHED0() __builtin_amdgcn_sched_barrier(0)

#define DMAF(m0v, voff, rs, soff) \
  asm volatile("s_mov_b32 m0, %0\n\ts_nop 0\n\tbuffer_load_dwordx4 %1, %2, %3 offen lds" ::"s"(m0v), "v"(voff), "s"(rs), "s"(soff) : "memory")
#define DMAFO(m0v, voff, rs, soff, OFF) \
  asm volatile("s_mov_b32 m0, %0\n\ts_nop 0\n\tbuffer_load_dwordx4 %1, %2, %3 offen offset:" #OFF " lds" ::"s"(m0v), "v"(voff), "s"(rs), "s"(soff) : "memory")
#define DMAN(voff, rs, soff, OFF) \
  asm volatile("buffer_load_dwordx4 %0, %1, %2 offen offset:" #OFF " lds" ::"v"(voff), "s"(rs), "s"(soff) : "memory")
#define DMAFS(m0v, voff, rs, soff) \
  asm volatile("s_mov_b32 m0, %0\n\ts_nop 0\n\tbuffer_load_dwordx4 %1, %2, %3 offen sc1 lds" ::"s"(m0v), "v"(voff), "s"(rs), "s"(soff) : "memory")
#define DMANS(voff, rs, soff, OFF) \
  asm volatile("buffer_load_dwordx4 %0, %1, %2 offen offset:" #OFF " sc1 lds" ::"v"(voff), "s"(rs), "s"(soff) : "memory")
#define LD128(dst, voff, rs, soff, OFF) \
  asm volatile("buffer_load_dwordx4 %0, %1, %2, %3 offen offset:" #OFF : "=v"(dst) : "v"(voff), "s"(rs), "s"(soff) : "memory")
#define ST32SC1(src, voff, rs) \
  asm volatile("buffer_store_dword %0, %1, %2, 0 offen sc1" ::"v"(src), "v"(voff), "s"(rs) : "memory")
#define WAITLGKM0B() __builtin_amdgcn_s_waitcnt(0xC07F)
#define WAITVMX(n) asm volatile("s_waitcnt vmcnt(%0)" ::"n"(n) : "memory")
typedef const __attribute__((address_space(4))) u32* cptr32;
#define LD128SC1(dst, voff, rs, soff, OFF) \
  asm volatile("buffer_load_dwordx4 %0, %1, %2, %3 offen offset:" #OFF " sc1" : "=v"(dst) : "v"(voff), "s"(rs), "s"(soff) : "memory")
#define LD64SC1(dst, voff, rs, soff, OFF) \
  asm volatile("buffer_load_dwordx2 %0, %1, %2, %3 offen offset:" #OFF " sc1" : "=v"(dst) : "v"(voff), "s"(rs), "s"(soff) : "memory")
#define LD64(dst, voff, rs, soff, OFF) \
  asm volatile("buffer_load_dwordx2 %0, %1, %2, %3 offen offset:" #OFF : "=v"(dst) : "v"(voff), "s"(rs), "s"(soff) : "memory")
#define LD32SC1(dst, voff, rs, soff) \
  asm volatile("buffer_load_dword %0, %1, %2, %3 offen sc1" : "=v"(dst) : "v"(voff), "s"(rs), "s"(soff) : "memory")
#define ST128SC1(src, voff, rs, soff, OFF) \
  asm volatile("buffer_store_dwordx4 %0, %1, %2, %3 offen offset:" #OFF " sc1\n\ts_nop 1" ::"v"(src), "v"(voff), "s"(rs), "s"(soff) : "memory")
#define PIN1(a) asm volatile("" : "+v"(a))

template <int SL = 1>
__device__ __forceinline__ void poll_flag(u32 voff, rsrc_t rs, u32 tag) {
  _Pragma("unroll 1") for (int sp = 0; sp < SPIN_MAX; ++sp) {
    u32 v;
    asm volatile("buffer_load_dword %0, %1, %2, 0 offen sc1\n\ts_waitcnt vmcnt(0)" : "=v"(v) : "v"(voff), "s"(rs) : "memory");
    if (__all(v == tag)) break;
    __builtin_amdgcn_s_sleep(SL);
  }
}

template <int CTRL>
__device__ __forceinline__ float dpp_add(float v) {
  return v + __int_as_float(__builtin_amdgcn_update_dpp(0, __float_as_int(v), CTRL, 0xf, 0xf, true));
}
__device__ __forceinline__ float red32(float v) {
  v = dpp_add<0xB1>(v);
  v = dpp_add<0x4E>(v);
  v = dpp_add<0x141>(v);
  v = dpp_add<0x140>(v);
  auto s = __builtin_amdgcn_permlane16_swap(__float_as_uint(v), __float_as_uint(v), false, false);
  return __uint_as_float(s[0]) + __uint_as_float(s[1]);
}
__device__ __forceinline__ u32 shl1(u32 v) { return (u32)__builtin_amdgcn_update_dpp(0, (int)v, 0x130, 0xf, 0xf, true); }
__device__ __forceinline__ u32 lohi(u32 lo, u32 hi) { return (lo & 0xffffu) | (hi & 0xffff0000u); }

__device__ __forceinline__ float dot2bf(u32 a, u32 b, float c) {
  return __builtin_amdgcn_fdot2_f32_bf16(__builtin_bit_cast(bf16x2, a), __builtin_bit_cast(bf16x2, b), c, false);
}
#define SHARE8 0
#define REMAP2 1
#define GELU_PK 0
#define ROT 0
#define NPF 3
__device__ __forceinline__ void gemm_role(const u16* x, const u16* w1, const u16* b2, u16* out, char* ws,
                                          LAS char* L, int b) {
  const int tid = threadIdx.x, lane = tid & 63;
  const int w = __builtin_amdgcn_readfirstlane(tid >> 6);
  const int xcd = b & 7, r = b >> 3;
  const int ti = (xcd >> 1) * 8 + (r >> 2), tj = (xcd & 1) * 4 + (r & 3);
  const int s2 = r < 16 ? r >> 3 : (r >= 20 && r < 28) ? 3 : 2;
  const int tj2 = s2 == 2 ? (r < 20 ? r - 16 : r - 24) : (r & 7);
  const int ti2 = (xcd >> 1) * 8 + ((((xcd & 1) ? 0x4327 : 0x1065) >> (4 * s2)) & 7);
  STAMPS_DECL
  STAMPR(0);
  const rsrc_t rx = mkrs(x, 2048 * 640 * 2), rw1 = mkrs((const char*)w1 - 2048, 640 * 1536 * 2 + 2048), rws = mkrs(ws, WS_SPAN);
  const rsrc_t rb2 = mkrs(b2, 640 * 2), rout = mkrs(out, 2048 * 640 * 2);
  const u32 lbase = (u32)(uintptr_t)L;
  const int wm = w & 1, wn = w >> 1;
  const int hl = lane >> 5, li = lane & 31, gl = (lane >> 4) & 1, q4 = (lane >> 2) & 3, p4 = lane & 3;
  u32 tag = *(cptr32)(uintptr_t)ws + TAG_ADD;
  u32 vx[2], vw[3];
  {
    const int cp = lane & 7, rr = lane >> 3;
#pragma unroll
    for (int h = 0; h < 2; ++h) vx[h] = (64 * ti + 16 * w + 8 * h + rr) * 1280 + ((cp ^ (4 * h + (rr >> 1))) << 4) - 1024 * h;
    const u32 sw = (cp ^ (((rr >> 1) & 1) << 2)) << 4;
#pragma unroll
    for (int p = 0; p < 3; ++p) vw[p] = (8 * w + rr) * 3072 + tj * 384 + sw + 2048 - 896 * p;
  }
  const u32 m0x = lbase + 49152 + w * 2048, m0w = lbase + w * 3072;
  DMAF(m0x + 0, vx[0], rx, 0);
  DMAFO(m0x + 0, vx[1], rx, 0, 1024);
  DMAF(m0w + 0, vw[0], rw1, 0);
  DMAN(vw[1], rw1, 0, 1024);
  DMAN(vw[2], rw1, 0, 2048);
  DMAF(m0w + 12288, vw[0], rw1, 98304);
  DMAN(vw[1], rw1, 98304, 1024);
  DMAN(vw[2], rw1, 98304, 2048);
  DMAF(m0x + 8192, vx[0], rx, 128);
  DMAFO(m0x + 8192, vx[1], rx, 128, 1024);
  DMAF(m0w + 24576, vw[0], rw1, 196608);
  DMAN(vw[1], rw1, 196608, 1024);
  DMAN(vw[2], rw1, 196608, 2048);
  DMAF(m0w + 36864, vw[0], rw1, 294912);
  DMAN(vw[1], rw1, 294912, 1024);
  DMAN(vw[2], rw1, 294912, 2048);
  DMAF(m0x + 16384, vx[0], rx, 256);
  DMAFO(m0x + 16384, vx[1], rx, 256, 1024);
  SCHED0();

#pragma unroll
  for (int u = 0; u < 3; ++u) {
    u32 lam = ((u * 32 + r) * 4 + w) * 32 + (lane >> 1);
    if (lam >= 10240) lam -= 4096;
    const u32 st = 2 + lam / 1280, wi = lam % 1280;
    const u32 y = wi - 512;
    const bool isx = wi < 512;
    const u32 off = isx ? ((xcd >> 1) * 512 + wi) * 1280 + st * 128 : (st * 64 + y / 12) * 3072 + (xcd & 1) * 1536 + (y % 12) * 128;
    const char* ptr = (isx ? (const char*)x : (const char*)w1) + off + (lane & 1) * 64;
    asm volatile("s_mov_b32 m0, %0\n\ts_nop 0\n\tglobal_load_lds_dword %1, off" ::"s"(lbase + 80896 + w * 256), "v"(ptr) : "memory");
  }
  LAS char* aw[6];
  LAS char* ax[4];
  {
    const int sw = (q4 >> 1) & 1;
#pragma unroll
    for (int k = 0; k < 6; ++k) {
      const int nf = 3 * wn + k % 3, ks = (k / 3) ^ wm;
      aw[k] = L + (2 * ks + hl) * 3072 + (nf >> 1) * 1024 + q4 * 128 + (((nf & 1) ^ sw) << 6) + gl * 32 + p4 * 8;
    }
    const int row = 32 * wm + li;
#pragma unroll
    for (int j = 0; j < 4; ++j) ax[j] = L + 49152 + row * 128 + (((2 * (j ^ wm) + hl) ^ ((row >> 1) & 7)) << 4);
  }
  f32x16 acc[3];
#pragma unroll
  for (int f = 0; f < 3; ++f) acc[f] = (f32x16)0.f;
  u32x4 csg[3];
  const u32 vcs = WS_CS + (192 * tj + 96 * wn + li) * 16;
  u32x2 stt;
  u32 w2fv;
  const u32 vw2f = WS_W2F + ((tj2 >> 1) * 32 + 8 * w + (lane & 7)) * 128;
  u32 kb[12];
#pragma unroll
  for (int j = 0; j < 12; ++j) {
    const int t = j + ROT * (ti2 & 3);
    kb[j] = 12 * w + (t >= 12 ? t - 12 : t);
  }
  const u32 w2base = WS_W2P + tj2 * 245760, m0p = lbase + w * 18432, m0q = lbase + 74752 + w * 1024;
  const u32 vst = WS_ST + (8 * ti + 4 * wm + (li >> 3)) * ST_STR + (li & 7) * 8;
  const u32 vl16 = lane * 16;
  asm volatile("" : "+s"(tag));
  u32x4 A0[6], A1[6], X0[2], X1[2];
  u32x4 W19, Wr[4][5];
  WAITLGKM0B();

  WAITVMX(13 + NPF);
  BAR();
  STAMPR(1);
  STAMPD(20);
  X0[0] = *(LAS u32x4*)(ax[0] + 0);
  X0[1] = *(LAS u32x4*)(ax[1] + 0);
  A0[0].xy = trrd(aw[0] + 0);
  A0[0].zw = trrd(aw[0] + 512);
  A0[1].xy = trrd(aw[1] + 0);
  A0[1].zw = trrd(aw[1] + 512);
  A0[2].xy = trrd(aw[2] + 0);
  A0[2].zw = trrd(aw[2] + 512);
  A0[3].xy = trrd(aw[3] + 0);
  A0[3].zw = trrd(aw[3] + 512);
  A0[4].xy = trrd(aw[4] + 0);
  A0[4].zw = trrd(aw[4] + 512);
  A0[5].xy = trrd(aw[5] + 0);
  A0[5].zw = trrd(aw[5] + 512);
  WAITVMX(10 + NPF);
  WAITLGKM0B();
  BAR();
  STAMPH(12);
  SCHED0();
  X1[0] = *(LAS u32x4*)(ax[2] + 0);
  X1[1] = *(LAS u32x4*)(ax[3] + 0);
  A1[0].xy = trrd(aw[0] + 12288);
  A1[0].zw = trrd(aw[0] + 12800);
  acc[0] = mfma32(A0[0], X0[0], acc[0]);
  DMAF(m0w + 0, vw[0], rw1, 393216);
  SCHED0();
  A1[1].xy = trrd(aw[1] + 12288);
  A1[1].zw = trrd(aw[1] + 12800);
  A1[2].xy = trrd(aw[2] + 12288);
  A1[2].zw = trrd(aw[2] + 12800);
  acc[1] = mfma32(A0[1], X0[0], acc[1]);
  SCHED0();
  A1[3].xy = trrd(aw[3] + 12288);
  A1[3].zw = trrd(aw[3] + 12800);
  A1[4].xy = trrd(aw[4] + 12288);
  acc[2] = mfma32(A0[2], X0[0], acc[2]);
  DMAN(vw[1], rw1, 393216, 1024);
  SCHED0();
  A1[4].zw = trrd(aw[4] + 12800);
  A1[5].xy = trrd(aw[5] + 12288);
  A1[5].zw = trrd(aw[5] + 12800);
  acc[0] = mfma32(A0[3], X0[1], acc[0]);
  SCHED0();
  acc[1] = mfma32(A0[4], X0[1], acc[1]);
  DMAN(vw[2], rw1, 393216, 2048);
  SCHED0();
  acc[2] = mfma32(A0[5], X0[1], acc[2]);
  SCHED0();
  WAITVMX(8 + NPF);
  WAITLGKM0B();
  BAR();
  SCHED0();
  X0[0] = *(LAS u32x4*)(ax[0] + 8192);
  X0[1] = *(LAS u32x4*)(ax[1] + 8192);
  A0[0].xy = trrd(aw[0] + 24576);
  A0[0].zw = trrd(aw[0] + 25088);
  acc[0] = mfma32(A1[0], X1[0], acc[0]);
  DMAF(m0w + 12288, vw[0], rw1, 491520);
  SCHED0();
  A0[1].xy = trrd(aw[1] + 24576);
  A0[1].zw = trrd(aw[1] + 25088);
  A0[2].xy = trrd(aw[2] + 24576);
  A0[2].zw = trrd(aw[2] + 25088);
  acc[1] = mfma32(A1[1], X1[0], acc[1]);
  SCHED0();
  A0[3].xy = trrd(aw[3] + 24576);
  A0[3].zw = trrd(aw[3] + 25088);
  A0[4].xy = trrd(aw[4] + 24576);
  acc[2] = mfma32(A1[2], X1[0], acc[2]);
  DMAN(vw[1], rw1, 491520, 1024);
  SCHED0();
  A0[4].zw = trrd(aw[4] + 25088);
  A0[5].xy = trrd(aw[5] + 24576);
  A0[5].zw = trrd(aw[5] + 25088);
  acc[0] = mfma32(A1[3], X1[1], acc[0]);
  DMAN(vw[2], rw1, 491520, 2048);
  SCHED0();
  acc[1] = mfma32(A1[4], X1[1], acc[1]);
  SCHED0();
  acc[2] = mfma32(A1[5], X1[1], acc[2]);
  DMAF(m0x + 0, vx[0], rx, 384);
  SCHED0();
  WAITVMX(9 + NPF);
  WAITLGKM0B();
  BAR();
  STAMPH(13);
  SCHED0();
  X1[0] = *(LAS u32x4*)(ax[2] + 8192);
  X1[1] = *(LAS u32x4*)(ax[3] + 8192);
  A1[0].xy = trrd(aw[0] + 36864);
  A1[0].zw = trrd(aw[0] + 37376);
  acc[0] = mfma32(A0[0], X0[0], acc[0]);
  DMAF(m0w + 24576, vw[0], rw1, 589824);
  SCHED0();
  A1[1].xy = trrd(aw[1] + 36864);
  A1[1].zw = trrd(aw[1] + 37376);
  A1[2].xy = trrd(aw[2] + 36864);
  A1[2].zw = trrd(aw[2] + 37376);
  acc[1] = mfma32(A0[1], X0[0], acc[1]);
  SCHED0();
  A1[3].xy = trrd(aw[3] + 36864);
  A1[3].zw = trrd(aw[3] + 37376);
  A1[4].xy = trrd(aw[4] + 36864);
  acc[2] = mfma32(A0[2], X0[0], acc[2]);
  DMAN(vw[1], rw1, 589824, 1024);
  SCHED0();
  A1[4].zw = trrd(aw[4] + 37376);
  A1[5].xy = trrd(aw[5] + 36864);
  A1[5].zw = trrd(aw[5] + 37376);
  acc[0] = mfma32(A0[3], X0[1], acc[0]);
  DMAN(vw[2], rw1, 589824, 2048);
  SCHED0();
  acc[1] = mfma32(A0[4], X0[1], acc[1]);
  SCHED0();
  acc[2] = mfma32(A0[5], X0[1], acc[2]);
  DMAFO(m0x + 0, vx[1], rx, 384, 1024);
  SCHED0();
  WAITVMX(8);
  WAITLGKM0B();
  BAR();
  SCHED0();
  X0[0] = *(LAS u32x4*)(ax[0] + 16384);
  X0[1] = *(LAS u32x4*)(ax[1] + 16384);
  A0[0].xy = trrd(aw[0] + 0);
  A0[0].zw = trrd(aw[0] + 512);
  acc[0] = mfma32(A1[0], X1[0], acc[0]);
  DMAF(m0w + 36864, vw[0], rw1, 688128);
  SCHED0();
  A0[1].xy = trrd(aw[1] + 0);
  A0[1].zw = trrd(aw[1] + 512);
  A0[2].xy = trrd(aw[2] + 0);
  A0[2].zw = trrd(aw[2] + 512);
  acc[1] = mfma32(A1[1], X1[0], acc[1]);
  SCHED0();
  A0[3].xy = trrd(aw[3] + 0);
  A0[3].zw = trrd(aw[3] + 512);
  A0[4].xy = trrd(aw[4] + 0);
  acc[2] = mfma32(A1[2], X1[0], acc[2]);
  DMAN(vw[1], rw1, 688128, 1024);
  SCHED0();
  A0[4].zw = trrd(aw[4] + 512);
  A0[5].xy = trrd(aw[5] + 0);
  A0[5].zw = trrd(aw[5] + 512);
  acc[0] = mfma32(A1[3], X1[1], acc[0]);
  DMAN(vw[2], rw1, 688128, 2048);
  SCHED0();
  acc[1] = mfma32(A1[4], X1[1], acc[1]);
  SCHED0();
  acc[2] = mfma32(A1[5], X1[1], acc[2]);
  DMAF(m0x + 8192, vx[0], rx, 512);
  SCHED0();
  WAITVMX(9);
  WAITLGKM0B();
  BAR();
  STAMPH(14);
  SCHED0();
  X1[0] = *(LAS u32x4*)(ax[2] + 16384);
  X1[1] = *(LAS u32x4*)(ax[3] + 16384);
  A1[0].xy = trrd(aw[0] + 12288);
  A1[0].zw = trrd(aw[0] + 12800);
  acc[0] = mfma32(A0[0], X0[0], acc[0]);
  DMAF(m0w + 0, vw[0], rw1, 786432);
  SCHED0();
  A1[1].xy = trrd(aw[1] + 12288);
  A1[1].zw = trrd(aw[1] + 12800);
  A1[2].xy = trrd(aw[2] + 12288);
  A1[2].zw = trrd(aw[2] + 12800);
  acc[1] = mfma32(A0[1], X0[0], acc[1]);
  SCHED0();
  A1[3].xy = trrd(aw[3] + 12288);
  A1[3].zw = trrd(aw[3] + 12800);
  A1[4].xy = trrd(aw[4] + 12288);
  acc[2] = mfma32(A0[2], X0[0], acc[2]);
  DMAN(vw[1], rw1, 786432, 1024);
  SCHED0();
  A1[4].zw = trrd(aw[4] + 12800);
  A1[5].xy = trrd(aw[5] + 12288);
  A1[5].zw = trrd(aw[5] + 12800);
  acc[0] = mfma32(A0[3], X0[1], acc[0]);
  DMAN(vw[2], rw1, 786432, 2048);
  SCHED0();
  acc[1] = mfma32(A0[4], X0[1], acc[1]);
  SCHED0();
  acc[2] = mfma32(A0[5], X0[1], acc[2]);
  DMAFO(m0x + 8192, vx[1], rx, 512, 1024);
  SCHED0();
  WAITVMX(8);
  WAITLGKM0B();
  BAR();
  SCHED0();
  X0[0] = *(LAS u32x4*)(ax[0] + 0);
  X0[1] = *(LAS u32x4*)(ax[1] + 0);
  A0[0].xy = trrd(aw[0] + 24576);
  A0[0].zw = trrd(aw[0] + 25088);
  acc[0] = mfma32(A1[0], X1[0], acc[0]);
  DMAF(m0w + 12288, vw[0], rw1, 884736);
  SCHED0();
  A0[1].xy = trrd(aw[1] + 24576);
  A0[1].zw = trrd(aw[1] + 25088);
  A0[2].xy = trrd(aw[2] + 24576);
  A0[2].zw = trrd(aw[2] + 25088);
  acc[1] = mfma32(A1[1], X1[0], acc[1]);
  SCHED0();
  A0[3].xy = trrd(aw[3] + 24576);
  A0[3].zw = trrd(aw[3] + 25088);
  A0[4].xy = trrd(aw[4] + 24576);
  acc[2] = mfma32(A1[2], X1[0], acc[2]);
  DMAN(vw[1], rw1, 884736, 1024);
  SCHED0();
  A0[4].zw = trrd(aw[4] + 25088);
  A0[5].xy = trrd(aw[5] + 24576);
  A0[5].zw = trrd(aw[5] + 25088);
  acc[0] = mfma32(A1[3], X1[1], acc[0]);
  DMAN(vw[2], rw1, 884736, 2048);
  SCHED0();
  acc[1] = mfma32(A1[4], X1[1], acc[1]);
  SCHED0();
  acc[2] = mfma32(A1[5], X1[1], acc[2]);
  DMAF(m0x + 16384, vx[0], rx, 640);
  SCHED0();
  WAITVMX(9);
  WAITLGKM0B();
  BAR();
  STAMPH(15);
  SCHED0();
  X1[0] = *(LAS u32x4*)(ax[2] + 0);
  X1[1] = *(LAS u32x4*)(ax[3] + 0);
  A1[0].xy = trrd(aw[0] + 36864);
  A1[0].zw = trrd(aw[0] + 37376);
  acc[0] = mfma32(A0[0], X0[0], acc[0]);
  DMAF(m0w + 24576, vw[0], rw1, 983040);
  SCHED0();
  A1[1].xy = trrd(aw[1] + 36864);
  A1[1].zw = trrd(aw[1] + 37376);
  A1[2].xy = trrd(aw[2] + 36864);
  A1[2].zw = trrd(aw[2] + 37376);
  acc[1] = mfma32(A0[1], X0[0], acc[1]);
  SCHED0();
  A1[3].xy = trrd(aw[3] + 36864);
  A1[3].zw = trrd(aw[3] + 37376);
  A1[4].xy = trrd(aw[4] + 36864);
  acc[2] = mfma32(A0[2], X0[0], acc[2]);
  DMAN(vw[1], rw1, 983040, 1024);
  SCHED0();
  A1[4].zw = trrd(aw[4] + 37376);
  A1[5].xy = trrd(aw[5] + 36864);
  A1[5].zw = trrd(aw[5] + 37376);
  acc[0] = mfma32(A0[3], X0[1], acc[0]);
  DMAN(vw[2], rw1, 983040, 2048);
  SCHED0();
  acc[1] = mfma32(A0[4], X0[1], acc[1]);
  SCHED0();
  acc[2] = mfma32(A0[5], X0[1], acc[2]);
  DMAFO(m0x + 16384, vx[1], rx, 640, 1024);
  SCHED0();
  WAITVMX(8);
  WAITLGKM0B();
  BAR();
  SCHED0();
  X0[0] = *(LAS u32x4*)(ax[0] + 8192);
  X0[1] = *(LAS u32x4*)(ax[1] + 8192);
  A0[0].xy = trrd(aw[0] + 0);
  A0[0].zw = trrd(aw[0] + 512);
  acc[0] = mfma32(A1[0], X1[0], acc[0]);
  DMAF(m0w + 36864, vw[0], rw1, 1081344);
  SCHED0();
  A0[1].xy = trrd(aw[1] + 0);
  A0[1].zw = trrd(aw[1] + 512);
  A0[2].xy = trrd(aw[2] + 0);
  A0[2].zw = trrd(aw[2] + 512);
  acc[1] = mfma32(A1[1], X1[0], acc[1]);
  SCHED0();
  A0[3].xy = trrd(aw[3] + 0);
  A0[3].zw = trrd(aw[3] + 512);
  A0[4].xy = trrd(aw[4] + 0);
  acc[2] = mfma32(A1[2], X1[0], acc[2]);
  DMAN(vw[1], rw1, 1081344, 1024);
  SCHED0();
  A0[4].zw = trrd(aw[4] + 512);
  A0[5].xy = trrd(aw[5] + 0);
  A0[5].zw = trrd(aw[5] + 512);
  acc[0] = mfma32(A1[3], X1[1], acc[0]);
  DMAN(vw[2], rw1, 1081344, 2048);
  SCHED0();
  acc[1] = mfma32(A1[4], X1[1], acc[1]);
  SCHED0();
  acc[2] = mfma32(A1[5], X1[1], acc[2]);
  DMAF(m0x + 0, vx[0], rx, 768);
  SCHED0();
  WAITVMX(9);
  WAITLGKM0B();
  BAR();
  STAMPH(16);
  SCHED0();
  X1[0] = *(LAS u32x4*)(ax[2] + 8192);
  X1[1] = *(LAS u32x4*)(ax[3] + 8192);
  A1[0].xy = trrd(aw[0] + 12288);
  A1[0].zw = trrd(aw[0] + 12800);
  acc[0] = mfma32(A0[0], X0[0], acc[0]);
  DMAF(m0w + 0, vw[0], rw1, 1179648);
  SCHED0();
  A1[1].xy = trrd(aw[1] + 12288);
  A1[1].zw = trrd(aw[1] + 12800);
  A1[2].xy = trrd(aw[2] + 12288);
  A1[2].zw = trrd(aw[2] + 12800);
  acc[1] = mfma32(A0[1], X0[0], acc[1]);
  SCHED0();
  A1[3].xy = trrd(aw[3] + 12288);
  A1[3].zw = trrd(aw[3] + 12800);
  A1[4].xy = trrd(aw[4] + 12288);
  acc[2] = mfma32(A0[2], X0[0], acc[2]);
  DMAN(vw[1], rw1, 1179648, 1024);
  SCHED0();
  A1[4].zw = trrd(aw[4] + 12800);
  A1[5].xy = trrd(aw[5] + 12288);
  A1[5].zw = trrd(aw[5] + 12800);
  acc[0] = mfma32(A0[3], X0[1], acc[0]);
  DMAN(vw[2], rw1, 1179648, 2048);
  SCHED0();
  acc[1] = mfma32(A0[4], X0[1], acc[1]);
  SCHED0();
  acc[2] = mfma32(A0[5], X0[1], acc[2]);
  DMAFO(m0x + 0, vx[1], rx, 768, 1024);
  SCHED0();
  WAITVMX(8);
  WAITLGKM0B();
  BAR();
  SCHED0();
  X0[0] = *(LAS u32x4*)(ax[0] + 16384);
  X0[1] = *(LAS u32x4*)(ax[1] + 16384);
  A0[0].xy = trrd(aw[0] + 24576);
  A0[0].zw = trrd(aw[0] + 25088);
  acc[0] = mfma32(A1[0], X1[0], acc[0]);
  DMAF(m0w + 12288, vw[0], rw1, 1277952);
  SCHED0();
  A0[1].xy = trrd(aw[1] + 24576);
  A0[1].zw = trrd(aw[1] + 25088);
  A0[2].xy = trrd(aw[2] + 24576);
  A0[2].zw = trrd(aw[2] + 25088);
  acc[1] = mfma32(A1[1], X1[0], acc[1]);
  SCHED0();
  A0[3].xy = trrd(aw[3] + 24576);
  A0[3].zw = trrd(aw[3] + 25088);
  A0[4].xy = trrd(aw[4] + 24576);
  acc[2] = mfma32(A1[2], X1[0], acc[2]);
  DMAN(vw[1], rw1, 1277952, 1024);
  SCHED0();
  A0[4].zw = trrd(aw[4] + 25088);
  A0[5].xy = trrd(aw[5] + 24576);
  A0[5].zw = trrd(aw[5] + 25088);
  acc[0] = mfma32(A1[3], X1[1], acc[0]);
  DMAN(vw[2], rw1, 1277952, 2048);
  SCHED0();
  acc[1] = mfma32(A1[4], X1[1], acc[1]);
  SCHED0();
  acc[2] = mfma32(A1[5], X1[1], acc[2]);
  DMAF(m0x + 8192, vx[0], rx, 896);
  SCHED0();
  WAITVMX(9);
  WAITLGKM0B();
  BAR();
  STAMPH(17);
  SCHED0();
  X1[0] = *(LAS u32x4*)(ax[2] + 16384);
  X1[1] = *(LAS u32x4*)(ax[3] + 16384);
  A1[0].xy = trrd(aw[0] + 36864);
  A1[0].zw = trrd(aw[0] + 37376);
  acc[0] = mfma32(A0[0], X0[0], acc[0]);
  DMAF(m0w + 24576, vw[0], rw1, 1376256);
  SCHED0();
  A1[1].xy = trrd(aw[1] + 36864);
  A1[1].zw = trrd(aw[1] + 37376);
  A1[2].xy = trrd(aw[2] + 36864);
  A1[2].zw = trrd(aw[2] + 37376);
  acc[1] = mfma32(A0[1], X0[0], acc[1]);
  SCHED0();
  A1[3].xy = trrd(aw[3] + 36864);
  A1[3].zw = trrd(aw[3] + 37376);
  A1[4].xy = trrd(aw[4] + 36864);
  acc[2] = mfma32(A0[2], X0[0], acc[2]);
  DMAN(vw[1], rw1, 1376256, 1024);
  SCHED0();
  A1[4].zw = trrd(aw[4] + 37376);
  A1[5].xy = trrd(aw[5] + 36864);
  A1[5].zw = trrd(aw[5] + 37376);
  acc[0] = mfma32(A0[3], X0[1], acc[0]);
  DMAN(vw[2], rw1, 1376256, 2048);
  SCHED0();
  acc[1] = mfma32(A0[4], X0[1], acc[1]);
  SCHED0();
  acc[2] = mfma32(A0[5], X0[1], acc[2]);
  DMAFO(m0x + 8192, vx[1], rx, 896, 1024);
  SCHED0();
  WAITVMX(8);
  WAITLGKM0B();
  BAR();
  SCHED0();
  X0[0] = *(LAS u32x4*)(ax[0] + 0);
  X0[1] = *(LAS u32x4*)(ax[1] + 0);
  A0[0].xy = trrd(aw[0] + 0);
  A0[0].zw = trrd(aw[0] + 512);
  acc[0] = mfma32(A1[0], X1[0], acc[0]);
  DMAF(m0w + 36864, vw[0], rw1, 1474560);
  SCHED0();
  A0[1].xy = trrd(aw[1] + 0);
  A0[1].zw = trrd(aw[1] + 512);
  A0[2].xy = trrd(aw[2] + 0);
  A0[2].zw = trrd(aw[2] + 512);
  acc[1] = mfma32(A1[1], X1[0], acc[1]);
  SCHED0();
  A0[3].xy = trrd(aw[3] + 0);
  A0[3].zw = trrd(aw[3] + 512);
  A0[4].xy = trrd(aw[4] + 0);
  acc[2] = mfma32(A1[2], X1[0], acc[2]);
  DMAN(vw[1], rw1, 1474560, 1024);
  SCHED0();
  A0[4].zw = trrd(aw[4] + 512);
  A0[5].xy = trrd(aw[5] + 0);
  A0[5].zw = trrd(aw[5] + 512);
  acc[0] = mfma32(A1[3], X1[1], acc[0]);
  DMAN(vw[2], rw1, 1474560, 2048);
  SCHED0();
  acc[1] = mfma32(A1[4], X1[1], acc[1]);
  SCHED0();
  acc[2] = mfma32(A1[5], X1[1], acc[2]);
  DMAF(m0x + 16384, vx[0], rx, 1024);
  SCHED0();
  WAITVMX(9);
  WAITLGKM0B();
  BAR();
  STAMPH(18);
  __builtin_amdgcn_s_setprio(PRIO_GM);
  SCHED0();
  SCHED0();
  X1[0] = *(LAS u32x4*)(ax[2] + 0);
  X1[1] = *(LAS u32x4*)(ax[3] + 0);
  A1[0].xy = trrd(aw[0] + 12288);
  A1[0].zw = trrd(aw[0] + 12800);
  acc[0] = mfma32(A0[0], X0[0], acc[0]);
  DMAF(m0w + 0, vw[0], rw1, 1572864);
  SCHED0();
  A1[1].xy = trrd(aw[1] + 12288);
  A1[1].zw = trrd(aw[1] + 12800);
  A1[2].xy = trrd(aw[2] + 12288);
  A1[2].zw = trrd(aw[2] + 12800);
  acc[1] = mfma32(A0[1], X0[0], acc[1]);
  SCHED0();
  A1[3].xy = trrd(aw[3] + 12288);
  A1[3].zw = trrd(aw[3] + 12800);
  A1[4].xy = trrd(aw[4] + 12288);
  acc[2] = mfma32(A0[2], X0[0], acc[2]);
  DMAN(vw[1], rw1, 1572864, 1024);
  SCHED0();
  A1[4].zw = trrd(aw[4] + 12800);
  A1[5].xy = trrd(aw[5] + 12288);
  A1[5].zw = trrd(aw[5] + 12800);
  acc[0] = mfma32(A0[3], X0[1], acc[0]);
  DMAN(vw[2], rw1, 1572864, 2048);
  SCHED0();
  acc[1] = mfma32(A0[4], X0[1], acc[1]);
  SCHED0();
  acc[2] = mfma32(A0[5], X0[1], acc[2]);
  DMAFO(m0x + 16384, vx[1], rx, 1024, 1024);
  SCHED0();
  WAITVMX(8);
  WAITLGKM0B();
  BAR();
  SCHED0();
  X0[0] = *(LAS u32x4*)(ax[0] + 8192);
  X0[1] = *(LAS u32x4*)(ax[1] + 8192);
  A0[0].xy = trrd(aw[0] + 24576);
  A0[0].zw = trrd(aw[0] + 25088);
  acc[0] = mfma32(A1[0], X1[0], acc[0]);
  DMAF(m0w + 12288, vw[0], rw1, 1671168);
  SCHED0();
  A0[1].xy = trrd(aw[1] + 24576);
  A0[1].zw = trrd(aw[1] + 25088);
  A0[2].xy = trrd(aw[2] + 24576);
  A0[2].zw = trrd(aw[2] + 25088);
  acc[1] = mfma32(A1[1], X1[0], acc[1]);
  SCHED0();
  A0[3].xy = trrd(aw[3] + 24576);
  A0[3].zw = trrd(aw[3] + 25088);
  A0[4].xy = trrd(aw[4] + 24576);
  acc[2] = mfma32(A1[2], X1[0], acc[2]);
  DMAN(vw[1], rw1, 1671168, 1024);
  SCHED0();
  A0[4].zw = trrd(aw[4] + 25088);
  A0[5].xy = trrd(aw[5] + 24576);
  A0[5].zw = trrd(aw[5] + 25088);
  acc[0] = mfma32(A1[3], X1[1], acc[0]);
  DMAN(vw[2], rw1, 1671168, 2048);
  SCHED0();
  acc[1] = mfma32(A1[4], X1[1], acc[1]);
  SCHED0();
  acc[2] = mfma32(A1[5], X1[1], acc[2]);
  DMAF(m0x + 0, vx[0], rx, 1152);
  SCHED0();
  WAITVMX(9);
  WAITLGKM0B();
  BAR();
  STAMPH(19);
  SCHED0();
  X1[0] = *(LAS u32x4*)(ax[2] + 8192);
  X1[1] = *(LAS u32x4*)(ax[3] + 8192);
  A1[0].xy = trrd(aw[0] + 36864);
  A1[0].zw = trrd(aw[0] + 37376);
  acc[0] = mfma32(A0[0], X0[0], acc[0]);
  DMAF(m0w + 24576, vw[0], rw1, 1769472);
  SCHED0();
  A1[1].xy = trrd(aw[1] + 36864);
  A1[1].zw = trrd(aw[1] + 37376);
  A1[2].xy = trrd(aw[2] + 36864);
  A1[2].zw = trrd(aw[2] + 37376);
  acc[1] = mfma32(A0[1], X0[0], acc[1]);
  SCHED0();
  A1[3].xy = trrd(aw[3] + 36864);
  A1[3].zw = trrd(aw[3] + 37376);
  A1[4].xy = trrd(aw[4] + 36864);
  acc[2] = mfma32(A0[2], X0[0], acc[2]);
  DMAN(vw[1], rw1, 1769472, 1024);
  SCHED0();
  A1[4].zw = trrd(aw[4] + 37376);
  A1[5].xy = trrd(aw[5] + 36864);
  A1[5].zw = trrd(aw[5] + 37376);
  acc[0] = mfma32(A0[3], X0[1], acc[0]);
  DMAN(vw[2], rw1, 1769472, 2048);
  SCHED0();
  acc[1] = mfma32(A0[4], X0[1], acc[1]);
  SCHED0();
  acc[2] = mfma32(A0[5], X0[1], acc[2]);
  DMAFO(m0x + 0, vx[1], rx, 1152, 1024);
  SCHED0();
  WAITVMX(8);
  WAITLGKM0B();
  BAR();
  SCHED0();
  X0[0] = *(LAS u32x4*)(ax[0] + 16384);
  X0[1] = *(LAS u32x4*)(ax[1] + 16384);
  A0[0].xy = trrd(aw[0] + 0);
  A0[0].zw = trrd(aw[0] + 512);
  acc[0] = mfma32(A1[0], X1[0], acc[0]);
  DMAF(m0w + 36864, vw[0], rw1, 1867776);
  SCHED0();
  A0[1].xy = trrd(aw[1] + 0);
  A0[1].zw = trrd(aw[1] + 512);
  A0[2].xy = trrd(aw[2] + 0);
  A0[2].zw = trrd(aw[2] + 512);
  acc[1] = mfma32(A1[1], X1[0], acc[1]);
  SCHED0();
  A0[3].xy = trrd(aw[3] + 0);
  A0[3].zw = trrd(aw[3] + 512);
  A0[4].xy = trrd(aw[4] + 0);
  acc[2] = mfma32(A1[2], X1[0], acc[2]);
  DMAN(vw[1], rw1, 1867776, 1024);
  SCHED0();
  A0[4].zw = trrd(aw[4] + 512);
  A0[5].xy = trrd(aw[5] + 0);
  A0[5].zw = trrd(aw[5] + 512);
  acc[0] = mfma32(A1[3], X1[1], acc[0]);
  SCHED0();
  acc[1] = mfma32(A1[4], X1[1], acc[1]);
  DMAN(vw[2], rw1, 1867776, 2048);
  SCHED0();
  acc[2] = mfma32(A1[5], X1[1], acc[2]);
  LD32SC1(w2fv, vw2f, rws, 0);
  LD64SC1(stt, vst, rws, 0, 0);
  LD128SC1(csg[0], vcs, rws, 0, 0);
  LD128SC1(csg[1], vcs, rws, 0, 512);
  LD128SC1(csg[2], vcs, rws, 0, 1024);
  SCHED0();
  WAITVMX(13);
  WAITLGKM0B();
  BAR();
  STAMPH(20);
  SCHED0();
  X1[0] = *(LAS u32x4*)(ax[2] + 16384);
  X1[1] = *(LAS u32x4*)(ax[3] + 16384);
  A1[0].xy = trrd(aw[0] + 12288);
  A1[0].zw = trrd(aw[0] + 12800);
  acc[0] = mfma32(A0[0], X0[0], acc[0]);
  SCHED0();
  A1[1].xy = trrd(aw[1] + 12288);
  A1[1].zw = trrd(aw[1] + 12800);
  A1[2].xy = trrd(aw[2] + 12288);
  A1[2].zw = trrd(aw[2] + 12800);
  acc[1] = mfma32(A0[1], X0[0], acc[1]);
  SCHED0();
  A1[3].xy = trrd(aw[3] + 12288);
  A1[3].zw = trrd(aw[3] + 12800);
  A1[4].xy = trrd(aw[4] + 12288);
  acc[2] = mfma32(A0[2], X0[0], acc[2]);
  SCHED0();
  A1[4].zw = trrd(aw[4] + 12800);
  A1[5].xy = trrd(aw[5] + 12288);
  A1[5].zw = trrd(aw[5] + 12800);
  acc[0] = mfma32(A0[3], X0[1], acc[0]);
  SCHED0();
  acc[1] = mfma32(A0[4], X0[1], acc[1]);
  SCHED0();
  acc[2] = mfma32(A0[5], X0[1], acc[2]);
  SCHED0();
  WAITVMX(8);
  WAITLGKM0B();
  BAR();
  SCHED0();
  X0[0] = *(LAS u32x4*)(ax[0] + 0);
  X0[1] = *(LAS u32x4*)(ax[1] + 0);
  A0[0].xy = trrd(aw[0] + 24576);
  A0[0].zw = trrd(aw[0] + 25088);
  acc[0] = mfma32(A1[0], X1[0], acc[0]);
  SCHED0();
  A0[1].xy = trrd(aw[1] + 24576);
  A0[1].zw = trrd(aw[1] + 25088);
  A0[2].xy = trrd(aw[2] + 24576);
  A0[2].zw = trrd(aw[2] + 25088);
  acc[1] = mfma32(A1[1], X1[0], acc[1]);
  SCHED0();
  A0[3].xy = trrd(aw[3] + 24576);
  A0[3].zw = trrd(aw[3] + 25088);
  A0[4].xy = trrd(aw[4] + 24576);
  acc[2] = mfma32(A1[2], X1[0], acc[2]);
  SCHED0();
  A0[4].zw = trrd(aw[4] + 25088);
  A0[5].xy = trrd(aw[5] + 24576);
  A0[5].zw = trrd(aw[5] + 25088);
  acc[0] = mfma32(A1[3], X1[1], acc[0]);
  SCHED0();
  acc[1] = mfma32(A1[4], X1[1], acc[1]);
  SCHED0();
  acc[2] = mfma32(A1[5], X1[1], acc[2]);
  SCHED0();
  WAITVMX(5);
  WAITLGKM0B();
  BAR();
  STAMPH(21);
  SCHED0();
  X1[0] = *(LAS u32x4*)(ax[2] + 0);
  X1[1] = *(LAS u32x4*)(ax[3] + 0);
  A1[0].xy = trrd(aw[0] + 36864);
  A1[0].zw = trrd(aw[0] + 37376);
  acc[0] = mfma32(A0[0], X0[0], acc[0]);
  SCHED0();
  A1[1].xy = trrd(aw[1] + 36864);
  A1[1].zw = trrd(aw[1] + 37376);
  A1[2].xy = trrd(aw[2] + 36864);
  A1[2].zw = trrd(aw[2] + 37376);
  acc[1] = mfma32(A0[1], X0[0], acc[1]);
  SCHED0();
  A1[3].xy = trrd(aw[3] + 36864);
  A1[3].zw = trrd(aw[3] + 37376);
  A1[4].xy = trrd(aw[4] + 36864);
  acc[2] = mfma32(A0[2], X0[0], acc[2]);
  SCHED0();
  A1[4].zw = trrd(aw[4] + 37376);
  A1[5].xy = trrd(aw[5] + 36864);
  A1[5].zw = trrd(aw[5] + 37376);
  acc[0] = mfma32(A0[3], X0[1], acc[0]);
  SCHED0();
  acc[1] = mfma32(A0[4], X0[1], acc[1]);
  SCHED0();
  acc[2] = mfma32(A0[5], X0[1], acc[2]);
  SCHED0();
  SCHED0();
  acc[0] = mfma32(A1[0], X1[0], acc[0]);
  SCHED0();
  acc[1] = mfma32(A1[1], X1[0], acc[1]);
  SCHED0();
  acc[2] = mfma32(A1[2], X1[0], acc[2]);
  SCHED0();
  acc[0] = mfma32(A1[3], X1[1], acc[0]);
  SCHED0();
  acc[1] = mfma32(A1[4], X1[1], acc[1]);
  SCHED0();
  acc[2] = mfma32(A1[5], X1[1], acc[2]);
  SCHED0();
  STAMPR(2);
  STAMPD(21);

  __builtin_amdgcn_s_setprio(PRIO_G2);
  __builtin_amdgcn_s_setprio(PRIO_G2);
  BAR();
  asm volatile("s_waitcnt vmcnt(%5)" : "+v"(stt), "+v"(w2fv), "+v"(csg[0]), "+v"(csg[1]), "+v"(csg[2]) : "n"(0) : "memory");
#define W2_LATE 1
  STAMPR(3);
  if (!__all(stt.y == tag)) {
    _Pragma("unroll 1") for (int sp = 0; sp < SPIN_MAX; ++sp) {
      asm volatile("buffer_load_dwordx2 %0, %1, %2, 0 offen sc1\n\ts_waitcnt vmcnt(0)" : "=v"(stt) : "v"(vst), "s"(rws) : "memory");
      if (__all(stt.y == tag)) break;
      __builtin_amdgcn_s_sleep(SL_W);
    }
  }
  if (!__all(((csg[0].w ^ tag) | (csg[1].w ^ tag) | (csg[2].w ^ tag)) == 0)) {
    __builtin_amdgcn_s_sleep(SL_W);
    _Pragma("unroll 1") for (int sp = 0; sp < SPIN_MAX; ++sp) {
      asm volatile("buffer_load_dwordx4 %0, %3, %4, 0 offen sc1\n\tbuffer_load_dwordx4 %1, %3, %4, 0 offen offset:512 sc1\n\t"
                   "buffer_load_dwordx4 %2, %3, %4, 0 offen offset:1024 sc1\n\ts_waitcnt vmcnt(0)"
                   : "=&v"(csg[0]), "=&v"(csg[1]), "=&v"(csg[2]) : "v"(vcs), "s"(rws) : "memory");
      if (__all(((csg[0].w ^ tag) | (csg[1].w ^ tag) | (csg[2].w ^ tag)) == 0)) break;
      __builtin_amdgcn_s_sleep(SL_W);
    }
  }
  {
    const u32 nm = (stt.x & 0xffffu) ^ 0x8000u;
    const u32x4 Bx = {hl ? 0u : nm * 0x10001u, hl ? 0u : stt.x >> 16, 0u, 0u};
#pragma unroll
    for (int f = 0; f < 3; ++f) acc[f] = mfma32((u32x4){hl ? 0u : csg[f].x, hl ? 0u : csg[f].y, 0u, 0u}, Bx, acc[f]);
  }
  STAMPR(4);
  if (!__all(w2fv == tag)) { __builtin_amdgcn_s_sleep(SL_W); poll_flag<SL_W>(vw2f, rws, tag); }
  {
    const float den = bfhi(stt.x), rr = __builtin_amdgcn_rcpf(den);
    const float C1 = -2.f * 1.4426950408889634f * 0.7978845608028654f, C2 = C1 * 0.044715f;
    const float K1 = C1 * rr, K2 = C2 * rr * rr * rr;
    const u32 hbase = WS_HP + ((ti * 48 + 6 * tj + 3 * wn) * 4 + 2 * wm) * 1024;
    u32x4 hv;
    float z0, z1, z2, z3, z4, z5, z6, z7;
#define R8(op) op(0, 8) op(1, 9) op(2, 10) op(3, 11) op(4, 12) op(5, 13) op(6, 14) op(7, 15)
#define GZO "=&v"(z0), "=&v"(z1), "=&v"(z2), "=&v"(z3), "=&v"(z4), "=&v"(z5), "=&v"(z6), "=&v"(z7)
#define GZB "+v"(z0), "+v"(z1), "+v"(z2), "+v"(z3), "+v"(z4), "+v"(z5), "+v"(z6), "+v"(z7)
#define GAI(f, bb) "v"(acc[f][8 * bb]), "v"(acc[f][8 * bb + 1]), "v"(acc[f][8 * bb + 2]), "v"(acc[f][8 * bb + 3]), "v"(acc[f][8 * bb + 4]), \
                   "v"(acc[f][8 * bb + 5]), "v"(acc[f][8 * bb + 6]), "v"(acc[f][8 * bb + 7])
#define OSQ(z, a) "v_mul_f32 %" #z ", %" #a ", %" #a "\n\t"
#define OPL(z, a) "v_fma_f32 %" #z ", %" #z ", %16, %17\n\t"
#define OML(z, a) "v_mul_f32 %" #z ", %" #z ", %" #a "\n\t"
#define OEX(z, a) "v_exp_f32 %" #z ", %" #z "\n\t"
#define ODN(z, a) "v_fma_f32 %" #z ", %" #z ", %8, %8\n\t"
#define ORC(z, a) "v_rcp_f32 %" #z ", %" #z "\n\t"
#define GELU_S0(f, bb) asm volatile(R8(OSQ) R8(OPL) R8(OML) : GZO : GAI(f, bb), "v"(K2), "v"(K1));
#define GELU_S1(f, bb) asm volatile(R8(OEX) R8(ODN) : GZB : "v"(den));
#define GELU_S2(f, bb) asm volatile(R8(ORC) : GZB);
#define GELU_S3(f, bb) \
  asm volatile(R8(OML) : GZB : GAI(f, bb)); \
  hv = (u32x4){pkbf(z0, z1), pkbf(z2, z3), pkbf(z4, z5), pkbf(z6, z7)};
    asm volatile("s_nop 15\n\ts_nop 15" : "+v"(acc[0]), "+v"(acc[1]), "+v"(acc[2]));
  DMAFS(m0p + 0, vl16, rws, w2base + kb[0] * 5120);
  DMANS(vl16, rws, w2base + kb[0] * 5120, 1024);
  DMANS(vl16, rws, w2base + kb[0] * 5120, 2048);
  DMANS(vl16, rws, w2base + kb[0] * 5120, 3072);
  DMAFS(m0p + 4096, vl16, rws, w2base + kb[0] * 5120 + 4096);
  DMAFS(m0p + 5120, vl16, rws, w2base + kb[1] * 5120);
  DMANS(vl16, rws, w2base + kb[1] * 5120, 1024);
  DMANS(vl16, rws, w2base + kb[1] * 5120, 2048);
    GELU_S0(0, 0)
  SCHED0();
  DMANS(vl16, rws, w2base + kb[1] * 5120, 3072);
  DMAFS(m0p + 9216, vl16, rws, w2base + kb[1] * 5120 + 4096);
  SCHED0();
    GELU_S1(0, 0)
  SCHED0();
  DMAFS(m0p + 10240, vl16, rws, w2base + kb[2] * 5120);
  DMANS(vl16, rws, w2base + kb[2] * 5120, 1024);
  SCHED0();
    GELU_S2(0, 0)
  SCHED0();
  DMANS(vl16, rws, w2base + kb[2] * 5120, 2048);
  DMANS(vl16, rws, w2base + kb[2] * 5120, 3072);
  SCHED0();
    GELU_S3(0, 0)
  SCHED0();
  DMAFS(m0p + 14336, vl16, rws, w2base + kb[2] * 5120 + 4096);
  SCHED0();
  STAMPH(28);
  ST128SC1(hv, vl16, rws, hbase + 0, 0);
  STAMPH(29);
  SCHED0();
    GELU_S0(0, 1)
  SCHED0();
  DMAFS(m0p + 15360, vl16, rws, w2base + kb[3] * 5120);
  DMANS(vl16, rws, w2base + kb[3] * 5120, 1024);
  SCHED0();
    GELU_S1(0, 1)
  SCHED0();
  DMANS(vl16, rws, w2base + kb[3] * 5120, 2048);
  DMAFS(m0q, vl16, rws, w2base + kb[3] * 5120 + 3072);
  SCHED0();
    GELU_S2(0, 1)
  SCHED0();
  LD128SC1(W19, vl16, rws, w2base + kb[3] * 5120 + 4096, 0);
  LD128SC1(Wr[0][0], vl16, rws, w2base + kb[4] * 5120, 0);
  SCHED0();
    GELU_S3(0, 1)
  SCHED0();
  LD128SC1(Wr[0][1], vl16, rws, w2base + kb[4] * 5120, 1024);
  SCHED0();
  STAMPH(30);
  ST128SC1(hv, vl16, rws, hbase + 0, 1024);
  SCHED0();
    GELU_S0(1, 0)
  SCHED0();
  LD128SC1(Wr[0][2], vl16, rws, w2base + kb[4] * 5120, 2048);
  LD128SC1(Wr[0][3], vl16, rws, w2base + kb[4] * 5120, 3072);
  SCHED0();
    GELU_S1(1, 0)
  SCHED0();
  LD128SC1(Wr[0][4], vl16, rws, w2base + kb[4] * 5120 + 4096, 0);
  LD128SC1(Wr[1][0], vl16, rws, w2base + kb[5] * 5120, 0);
  SCHED0();
    GELU_S2(1, 0)
  SCHED0();
  LD128SC1(Wr[1][1], vl16, rws, w2base + kb[5] * 5120, 1024);
  SCHED0();
    GELU_S3(1, 0)
  SCHED0();
  LD128SC1(Wr[1][2], vl16, rws, w2base + kb[5] * 5120, 2048);
  SCHED0();
  ST128SC1(hv, vl16, rws, hbase + 4096, 0);
  SCHED0();
    GELU_S0(1, 1)
  SCHED0();
  LD128SC1(Wr[1][3], vl16, rws, w2base + kb[5] * 5120, 3072);
  LD128SC1(Wr[1][4], vl16, rws, w2base + kb[5] * 5120 + 4096, 0);
  SCHED0();
    GELU_S1(1, 1)
  SCHED0();
  LD128SC1(Wr[2][0], vl16, rws, w2base + kb[6] * 5120, 0);
  LD128SC1(Wr[2][1], vl16, rws, w2base + kb[6] * 5120, 1024);
  SCHED0();
    GELU_S2(1, 1)
  SCHED0();
  LD128SC1(Wr[2][2], vl16, rws, w2base + kb[6] * 5120, 2048);
  SCHED0();
    GELU_S3(1, 1)
  SCHED0();
  LD128SC1(Wr[2][3], vl16, rws, w2base + kb[6] * 5120, 3072);
  SCHED0();
  ST128SC1(hv, vl16, rws, hbase + 4096, 1024);
  SCHED0();
    GELU_S0(2, 0)
  SCHED0();
  LD128SC1(Wr[2][4], vl16, rws, w2base + kb[6] * 5120 + 4096, 0);
  LD128SC1(Wr[3][0], vl16, rws, w2base + kb[7] * 5120, 0);
  SCHED0();
    GELU_S1(2, 0)
  SCHED0();
  LD128SC1(Wr[3][1], vl16, rws, w2base + kb[7] * 5120, 1024);
  LD128SC1(Wr[3][2], vl16, rws, w2base + kb[7] * 5120, 2048);
  SCHED0();
    GELU_S2(2, 0)
  SCHED0();
  LD128SC1(Wr[3][3], vl16, rws, w2base + kb[7] * 5120, 3072);
  SCHED0();
    GELU_S3(2, 0)
  SCHED0();
  LD128SC1(Wr[3][4], vl16, rws, w2base + kb[7] * 5120 + 4096, 0);
  SCHED0();
  ST128SC1(hv, vl16, rws, hbase + 8192, 0);
  SCHED0();
    GELU_S0(2, 1)
  SCHED0();
  SCHED0();
    GELU_S1(2, 1)
  SCHED0();
  SCHED0();
    GELU_S2(2, 1)
  SCHED0();
  SCHED0();
    GELU_S3(2, 1)
  SCHED0();
  SCHED0();
  ST128SC1(hv, vl16, rws, hbase + 8192, 1024);
  SCHED0();
  STAMPR(5);
  __builtin_amdgcn_s_setprio(PRIO_G3);
  }
  u32x4 H[4][4];
  u32 hrd[4];
#pragma unroll
  for (int ml = 0; ml < 4; ++ml) {
    const int mf = (ml + w) & 3;
    hrd[ml] = WS_HP + ti2 * 196608 + (mf >> 1) * 2048 + (mf & 1) * 256;
  }
  const int g = lane >> 4, m16 = lane & 15;
  const u32 vh = (g >> 1) * 1024 + (g & 1) * 512 + m16 * 16;
  const f32x4 zero4 = {0.f, 0.f, 0.f, 0.f};
  f32x4 o[5][4];
  LAS char* wl = L + w * 18432 + lane * 16;
  LAS char* wq = L + 74752 + w * 1024 + lane * 16;
  u32x2 xr[5], b2r[5], sr[5];
  u32 sfv;
  const u32 vflag = WS_HF + (ti * 8 + tj) * 128 + (w == 0 ? lane * 4 : 0x40000000);
#define FH_STORE BAR(); if (lane < 4) ST32SC1(tag, vflag, rws);
  const u32 vhf = WS_HF + (ti2 * 8 + 2 * w + (lane & 1)) * 128 + ((lane >> 1) & 3) * 4;
  const u32 vxo = (64 * ti2 + 16 * w + m16) * 1280 + (80 * tj2 + 4 * g) * 2;
  const u32 vb2 = (80 * tj2 + 4 * g) * 2;
  const u32 vsf = WS_SF + (20 * tj2 + (lane < 20 ? lane : 19)) * 128 + (ti2 >> 3) * SF_STRIDE;
  const u32 vsp = WS_SP + (20 * tj2 + g) * 16384 + (64 * ti2 + 16 * w + m16) * 8;
  WAITVMX(0);
  FH_STORE
  STAMPR(6);
  poll_flag<SL_H>(vhf, rws, tag);
  asm volatile("" : "+v"(W19), "+v"(Wr[0][0]), "+v"(Wr[0][1]), "+v"(Wr[0][2]), "+v"(Wr[0][3]), "+v"(Wr[0][4]), "+v"(Wr[1][0]), "+v"(Wr[1][1]), "+v"(Wr[1][2]), "+v"(Wr[1][3]), "+v"(Wr[1][4]));
  asm volatile("" : "+v"(Wr[2][0]), "+v"(Wr[2][1]), "+v"(Wr[2][2]), "+v"(Wr[2][3]), "+v"(Wr[2][4]), "+v"(Wr[3][0]), "+v"(Wr[3][1]), "+v"(Wr[3][2]), "+v"(Wr[3][3]), "+v"(Wr[3][4]));
  STAMPR(7);
  STAMPD(22);
  LD128SC1(H[0][0], vh, rws, hrd[0] + kb[4] * 4096, 0);
  LD128SC1(H[0][1], vh, rws, hrd[1] + kb[4] * 4096, 0);
  LD128SC1(H[0][2], vh, rws, hrd[2] + kb[4] * 4096, 0);
  LD128SC1(H[0][3], vh, rws, hrd[3] + kb[4] * 4096, 0);
  LD128SC1(H[1][0], vh, rws, hrd[0] + kb[5] * 4096, 0);
  LD128SC1(H[1][1], vh, rws, hrd[1] + kb[5] * 4096, 0);
  LD128SC1(H[1][2], vh, rws, hrd[2] + kb[5] * 4096, 0);
  LD128SC1(H[1][3], vh, rws, hrd[3] + kb[5] * 4096, 0);
  LD128SC1(H[2][0], vh, rws, hrd[0] + kb[6] * 4096, 0);
  LD128SC1(H[2][1], vh, rws, hrd[1] + kb[6] * 4096, 0);
  LD128SC1(H[2][2], vh, rws, hrd[2] + kb[6] * 4096, 0);
  LD128SC1(H[2][3], vh, rws, hrd[3] + kb[6] * 4096, 0);
  LD128SC1(H[3][0], vh, rws, hrd[0] + kb[7] * 4096, 0);
  LD128SC1(H[3][1], vh, rws, hrd[1] + kb[7] * 4096, 0);
  LD128SC1(H[3][2], vh, rws, hrd[2] + kb[7] * 4096, 0);
  LD128SC1(H[3][3], vh, rws, hrd[3] + kb[7] * 4096, 0);
  asm volatile("s_waitcnt vmcnt(%1)" : "+v"(H[0][0]) : "n"(15) : "memory");
  o[0][0] = mfma16(Wr[0][0], H[0][0], zero4);
  o[1][0] = mfma16(Wr[0][1], H[0][0], zero4);
  o[2][0] = mfma16(Wr[0][2], H[0][0], zero4);
  o[3][0] = mfma16(Wr[0][3], H[0][0], zero4);
  o[4][0] = mfma16(Wr[0][4], H[0][0], zero4);
  SCHED0();
  LD128SC1(H[0][0], vh, rws, hrd[0] + kb[0] * 4096, 0);
  asm volatile("s_waitcnt vmcnt(%1)" : "+v"(H[0][1]) : "n"(15) : "memory");
  o[0][1] = mfma16(Wr[0][0], H[0][1], zero4);
  o[1][1] = mfma16(Wr[0][1], H[0][1], zero4);
  o[2][1] = mfma16(Wr[0][2], H[0][1], zero4);
  o[3][1] = mfma16(Wr[0][3], H[0][1], zero4);
  o[4][1] = mfma16(Wr[0][4], H[0][1], zero4);
  SCHED0();
  LD128SC1(H[0][1], vh, rws, hrd[1] + kb[0] * 4096, 0);
  asm volatile("s_waitcnt vmcnt(%1)" : "+v"(H[0][2]) : "n"(15) : "memory");
  o[0][2] = mfma16(Wr[0][0], H[0][2], zero4);
  o[1][2] = mfma16(Wr[0][1], H[0][2], zero4);
  o[2][2] = mfma16(Wr[0][2], H[0][2], zero4);
  o[3][2] = mfma16(Wr[0][3], H[0][2], zero4);
  o[4][2] = mfma16(Wr[0][4], H[0][2], zero4);
  SCHED0();
  LD128SC1(H[0][2], vh, rws, hrd[2] + kb[0] * 4096, 0);
  asm volatile("s_waitcnt vmcnt(%1)" : "+v"(H[0][3]) : "n"(15) : "memory");
  o[0][3] = mfma16(Wr[0][0], H[0][3], zero4);
  o[1][3] = mfma16(Wr[0][1], H[0][3], zero4);
  o[2][3] = mfma16(Wr[0][2], H[0][3], zero4);
  o[3][3] = mfma16(Wr[0][3], H[0][3], zero4);
  o[4][3] = mfma16(Wr[0][4], H[0][3], zero4);
  SCHED0();
  LD128SC1(H[0][3], vh, rws, hrd[3] + kb[0] * 4096, 0);
  asm volatile("s_waitcnt vmcnt(%1)" : "+v"(H[1][0]) : "n"(15) : "memory");
  o[0][0] = mfma16(Wr[1][0], H[1][0], o[0][0]);
  o[1][0] = mfma16(Wr[1][1], H[1][0], o[1][0]);
  o[2][0] = mfma16(Wr[1][2], H[1][0], o[2][0]);
  o[3][0] = mfma16(Wr[1][3], H[1][0], o[3][0]);
  o[4][0] = mfma16(Wr[1][4], H[1][0], o[4][0]);
  SCHED0();
  LD128SC1(H[1][0], vh, rws, hrd[0] + kb[1] * 4096, 0);
  LD128SC1(Wr[0][0], vl16, rws, w2base + kb[8] * 5120, 0);
  LD128SC1(Wr[0][1], vl16, rws, w2base + kb[8] * 5120, 1024);
  asm volatile("s_waitcnt vmcnt(%1)" : "+v"(H[1][1]) : "n"(17) : "memory");
  o[0][1] = mfma16(Wr[1][0], H[1][1], o[0][1]);
  o[1][1] = mfma16(Wr[1][1], H[1][1], o[1][1]);
  o[2][1] = mfma16(Wr[1][2], H[1][1], o[2][1]);
  o[3][1] = mfma16(Wr[1][3], H[1][1], o[3][1]);
  o[4][1] = mfma16(Wr[1][4], H[1][1], o[4][1]);
  SCHED0();
  LD128SC1(H[1][1], vh, rws, hrd[1] + kb[1] * 4096, 0);
  LD128SC1(Wr[0][2], vl16, rws, w2base + kb[8] * 5120, 2048);
  asm volatile("s_waitcnt vmcnt(%1)" : "+v"(H[1][2]) : "n"(18) : "memory");
  o[0][2] = mfma16(Wr[1][0], H[1][2], o[0][2]);
  o[1][2] = mfma16(Wr[1][1], H[1][2], o[1][2]);
  o[2][2] = mfma16(Wr[1][2], H[1][2], o[2][2]);
  o[3][2] = mfma16(Wr[1][3], H[1][2], o[3][2]);
  o[4][2] = mfma16(Wr[1][4], H[1][2], o[4][2]);
  SCHED0();
  LD128SC1(H[1][2], vh, rws, hrd[2] + kb[1] * 4096, 0);
  LD128SC1(Wr[0][3], vl16, rws, w2base + kb[8] * 5120, 3072);
  asm volatile("s_waitcnt vmcnt(%1)" : "+v"(H[1][3]) : "n"(19) : "memory");
  o[0][3] = mfma16(Wr[1][0], H[1][3], o[0][3]);
  o[1][3] = mfma16(Wr[1][1], H[1][3], o[1][3]);
  o[2][3] = mfma16(Wr[1][2], H[1][3], o[2][3]);
  o[3][3] = mfma16(Wr[1][3], H[1][3], o[3][3]);
  o[4][3] = mfma16(Wr[1][4], H[1][3], o[4][3]);
  SCHED0();
  LD128SC1(H[1][3], vh, rws, hrd[3] + kb[1] * 4096, 0);
  LD128SC1(Wr[0][4], vl16, rws, w2base + kb[8] * 5120 + 4096, 0);
  STAMPH(23);
  asm volatile("s_waitcnt vmcnt(%1)" : "+v"(H[2][0]) : "n"(20) : "memory");
  o[0][0] = mfma16(Wr[2][0], H[2][0], o[0][0]);
  o[1][0] = mfma16(Wr[2][1], H[2][0], o[1][0]);
  o[2][0] = mfma16(Wr[2][2], H[2][0], o[2][0]);
  o[3][0] = mfma16(Wr[2][3], H[2][0], o[3][0]);
  o[4][0] = mfma16(Wr[2][4], H[2][0], o[4][0]);
  SCHED0();
  LD128SC1(H[2][0], vh, rws, hrd[0] + kb[2] * 4096, 0);
  LD128SC1(Wr[1][0], vl16, rws, w2base + kb[9] * 5120, 0);
  LD128SC1(Wr[1][1], vl16, rws, w2base + kb[9] * 5120, 1024);
  asm volatile("s_waitcnt vmcnt(%1)" : "+v"(H[2][1]) : "n"(22) : "memory");
  o[0][1] = mfma16(Wr[2][0], H[2][1], o[0][1]);
  o[1][1] = mfma16(Wr[2][1], H[2][1], o[1][1]);
  o[2][1] = mfma16(Wr[2][2], H[2][1], o[2][1]);
  o[3][1] = mfma16(Wr[2][3], H[2][1], o[3][1]);
  o[4][1] = mfma16(Wr[2][4], H[2][1], o[4][1]);
  SCHED0();
  LD128SC1(H[2][1], vh, rws, hrd[1] + kb[2] * 4096, 0);
  LD128SC1(Wr[1][2], vl16, rws, w2base + kb[9] * 5120, 2048);
  asm volatile("s_waitcnt vmcnt(%1)" : "+v"(H[2][2]) : "n"(23) : "memory");
  o[0][2] = mfma16(Wr[2][0], H[2][2], o[0][2]);
  o[1][2] = mfma16(Wr[2][1], H[2][2], o[1][2]);
  o[2][2] = mfma16(Wr[2][2], H[2][2], o[2][2]);
  o[3][2] = mfma16(Wr[2][3], H[2][2], o[3][2]);
  o[4][2] = mfma16(Wr[2][4], H[2][2], o[4][2]);
  SCHED0();
  LD128SC1(H[2][2], vh, rws, hrd[2] + kb[2] * 4096, 0);
  LD128SC1(Wr[1][3], vl16, rws, w2base + kb[9] * 5120, 3072);
  asm volatile("s_waitcnt vmcnt(%1)" : "+v"(H[2][3]) : "n"(24) : "memory");
  o[0][3] = mfma16(Wr[2][0], H[2][3], o[0][3]);
  o[1][3] = mfma16(Wr[2][1], H[2][3], o[1][3]);
  o[2][3] = mfma16(Wr[2][2], H[2][3], o[2][3]);
  o[3][3] = mfma16(Wr[2][3], H[2][3], o[3][3]);
  o[4][3] = mfma16(Wr[2][4], H[2][3], o[4][3]);
  SCHED0();
  LD128SC1(H[2][3], vh, rws, hrd[3] + kb[2] * 4096, 0);
  LD128SC1(Wr[1][4], vl16, rws, w2base + kb[9] * 5120 + 4096, 0);
  asm volatile("s_waitcnt vmcnt(%1)" : "+v"(H[3][0]) : "n"(25) : "memory");
  Wr[2][0] = *(LAS u32x4*)(wl + 0);
  Wr[2][1] = *(LAS u32x4*)(wl + 1024);
  Wr[2][2] = *(LAS u32x4*)(wl + 2048);
  Wr[2][3] = *(LAS u32x4*)(wl + 3072);
  Wr[2][4] = *(LAS u32x4*)(wl + 4096);
  o[0][0] = mfma16(Wr[3][0], H[3][0], o[0][0]);
  o[1][0] = mfma16(Wr[3][1], H[3][0], o[1][0]);
  o[2][0] = mfma16(Wr[3][2], H[3][0], o[2][0]);
  o[3][0] = mfma16(Wr[3][3], H[3][0], o[3][0]);
  o[4][0] = mfma16(Wr[3][4], H[3][0], o[4][0]);
  SCHED0();
  LD128SC1(H[3][0], vh, rws, hrd[0] + kb[3] * 4096, 0);
  asm volatile("s_waitcnt vmcnt(%1)" : "+v"(H[3][1]) : "n"(25) : "memory");
  o[0][1] = mfma16(Wr[3][0], H[3][1], o[0][1]);
  o[1][1] = mfma16(Wr[3][1], H[3][1], o[1][1]);
  o[2][1] = mfma16(Wr[3][2], H[3][1], o[2][1]);
  o[3][1] = mfma16(Wr[3][3], H[3][1], o[3][1]);
  o[4][1] = mfma16(Wr[3][4], H[3][1], o[4][1]);
  SCHED0();
  LD128SC1(H[3][1], vh, rws, hrd[1] + kb[3] * 4096, 0);
  asm volatile("s_waitcnt vmcnt(%1)" : "+v"(H[3][2]) : "n"(25) : "memory");
  o[0][2] = mfma16(Wr[3][0], H[3][2], o[0][2]);
  o[1][2] = mfma16(Wr[3][1], H[3][2], o[1][2]);
  o[2][2] = mfma16(Wr[3][2], H[3][2], o[2][2]);
  o[3][2] = mfma16(Wr[3][3], H[3][2], o[3][2]);
  o[4][2] = mfma16(Wr[3][4], H[3][2], o[4][2]);
  SCHED0();
  LD128SC1(H[3][2], vh, rws, hrd[2] + kb[3] * 4096, 0);
  asm volatile("s_waitcnt vmcnt(%1)" : "+v"(H[3][3]) : "n"(25) : "memory");
  o[0][3] = mfma16(Wr[3][0], H[3][3], o[0][3]);
  o[1][3] = mfma16(Wr[3][1], H[3][3], o[1][3]);
  o[2][3] = mfma16(Wr[3][2], H[3][3], o[2][3]);
  o[3][3] = mfma16(Wr[3][3], H[3][3], o[3][3]);
  o[4][3] = mfma16(Wr[3][4], H[3][3], o[4][3]);
  SCHED0();
  LD128SC1(H[3][3], vh, rws, hrd[3] + kb[3] * 4096, 0);
  STAMPH(24);
  asm volatile("s_waitcnt vmcnt(%1)" : "+v"(H[0][0]) : "n"(25) : "memory");
  Wr[3][0] = *(LAS u32x4*)(wl + 5120);
  Wr[3][1] = *(LAS u32x4*)(wl + 6144);
  Wr[3][2] = *(LAS u32x4*)(wl + 7168);
  Wr[3][3] = *(LAS u32x4*)(wl + 8192);
  Wr[3][4] = *(LAS u32x4*)(wl + 9216);
  o[0][0] = mfma16(Wr[2][0], H[0][0], o[0][0]);
  o[1][0] = mfma16(Wr[2][1], H[0][0], o[1][0]);
  o[2][0] = mfma16(Wr[2][2], H[0][0], o[2][0]);
  o[3][0] = mfma16(Wr[2][3], H[0][0], o[3][0]);
  o[4][0] = mfma16(Wr[2][4], H[0][0], o[4][0]);
  SCHED0();
  LD128SC1(H[0][0], vh, rws, hrd[0] + kb[8] * 4096, 0);
  asm volatile("s_waitcnt vmcnt(%1)" : "+v"(H[0][1]) : "n"(25) : "memory");
  o[0][1] = mfma16(Wr[2][0], H[0][1], o[0][1]);
  o[1][1] = mfma16(Wr[2][1], H[0][1], o[1][1]);
  o[2][1] = mfma16(Wr[2][2], H[0][1], o[2][1]);
  o[3][1] = mfma16(Wr[2][3], H[0][1], o[3][1]);
  o[4][1] = mfma16(Wr[2][4], H[0][1], o[4][1]);
  SCHED0();
  LD128SC1(H[0][1], vh, rws, hrd[1] + kb[8] * 4096, 0);
  asm volatile("s_waitcnt vmcnt(%1)" : "+v"(H[0][2]) : "n"(25) : "memory");
  o[0][2] = mfma16(Wr[2][0], H[0][2], o[0][2]);
  o[1][2] = mfma16(Wr[2][1], H[0][2], o[1][2]);
  o[2][2] = mfma16(Wr[2][2], H[0][2], o[2][2]);
  o[3][2] = mfma16(Wr[2][3], H[0][2], o[3][2]);
  o[4][2] = mfma16(Wr[2][4], H[0][2], o[4][2]);
  SCHED0();
  LD128SC1(H[0][2], vh, rws, hrd[2] + kb[8] * 4096, 0);
  asm volatile("s_waitcnt vmcnt(%1)" : "+v"(H[0][3]) : "n"(25) : "memory");
  o[0][3] = mfma16(Wr[2][0], H[0][3], o[0][3]);
  o[1][3] = mfma16(Wr[2][1], H[0][3], o[1][3]);
  o[2][3] = mfma16(Wr[2][2], H[0][3], o[2][3]);
  o[3][3] = mfma16(Wr[2][3], H[0][3], o[3][3]);
  o[4][3] = mfma16(Wr[2][4], H[0][3], o[4][3]);
  SCHED0();
  LD128SC1(H[0][3], vh, rws, hrd[3] + kb[8] * 4096, 0);
  asm volatile("s_waitcnt vmcnt(%1)" : "+v"(H[1][0]) : "n"(25) : "memory");
  Wr[2][0] = *(LAS u32x4*)(wl + 10240);
  Wr[2][1] = *(LAS u32x4*)(wl + 11264);
  Wr[2][2] = *(LAS u32x4*)(wl + 12288);
  Wr[2][3] = *(LAS u32x4*)(wl + 13312);
  Wr[2][4] = *(LAS u32x4*)(wl + 14336);
  o[0][0] = mfma16(Wr[3][0], H[1][0], o[0][0]);
  o[1][0] = mfma16(Wr[3][1], H[1][0], o[1][0]);
  o[2][0] = mfma16(Wr[3][2], H[1][0], o[2][0]);
  o[3][0] = mfma16(Wr[3][3], H[1][0], o[3][0]);
  o[4][0] = mfma16(Wr[3][4], H[1][0], o[4][0]);
  SCHED0();
  LD128SC1(H[1][0], vh, rws, hrd[0] + kb[9] * 4096, 0);
  asm volatile("s_waitcnt vmcnt(%1)" : "+v"(H[1][1]) : "n"(23) : "memory");
  o[0][1] = mfma16(Wr[3][0], H[1][1], o[0][1]);
  o[1][1] = mfma16(Wr[3][1], H[1][1], o[1][1]);
  o[2][1] = mfma16(Wr[3][2], H[1][1], o[2][1]);
  o[3][1] = mfma16(Wr[3][3], H[1][1], o[3][1]);
  o[4][1] = mfma16(Wr[3][4], H[1][1], o[4][1]);
  SCHED0();
  LD128SC1(H[1][1], vh, rws, hrd[1] + kb[9] * 4096, 0);
  asm volatile("s_waitcnt vmcnt(%1)" : "+v"(H[1][2]) : "n"(22) : "memory");
  o[0][2] = mfma16(Wr[3][0], H[1][2], o[0][2]);
  o[1][2] = mfma16(Wr[3][1], H[1][2], o[1][2]);
  o[2][2] = mfma16(Wr[3][2], H[1][2], o[2][2]);
  o[3][2] = mfma16(Wr[3][3], H[1][2], o[3][2]);
  o[4][2] = mfma16(Wr[3][4], H[1][2], o[4][2]);
  SCHED0();
  LD128SC1(H[1][2], vh, rws, hrd[2] + kb[9] * 4096, 0);
  asm volatile("s_waitcnt vmcnt(%1)" : "+v"(H[1][3]) : "n"(21) : "memory");
  o[0][3] = mfma16(Wr[3][0], H[1][3], o[0][3]);
  o[1][3] = mfma16(Wr[3][1], H[1][3], o[1][3]);
  o[2][3] = mfma16(Wr[3][2], H[1][3], o[2][3]);
  o[3][3] = mfma16(Wr[3][3], H[1][3], o[3][3]);
  o[4][3] = mfma16(Wr[3][4], H[1][3], o[4][3]);
  SCHED0();
  LD128SC1(H[1][3], vh, rws, hrd[3] + kb[9] * 4096, 0);
  STAMPH(25);
  asm volatile("s_waitcnt vmcnt(%1)" : "+v"(H[2][0]) : "n"(20) : "memory");
  Wr[3][0] = *(LAS u32x4*)(wl + 15360);
  Wr[3][1] = *(LAS u32x4*)(wl + 16384);
  Wr[3][2] = *(LAS u32x4*)(wl + 17408);
  Wr[3][3] = *(LAS u32x4*)(wq);
  Wr[3][4] = W19;
  o[0][0] = mfma16(Wr[2][0], H[2][0], o[0][0]);
  o[1][0] = mfma16(Wr[2][1], H[2][0], o[1][0]);
  o[2][0] = mfma16(Wr[2][2], H[2][0], o[2][0]);
  o[3][0] = mfma16(Wr[2][3], H[2][0], o[3][0]);
  o[4][0] = mfma16(Wr[2][4], H[2][0], o[4][0]);
  SCHED0();
  LD128SC1(H[2][0], vh, rws, hrd[0] + kb[10] * 4096, 0);
  asm volatile("s_waitcnt vmcnt(%1)" : "+v"(H[2][1]) : "n"(18) : "memory");
  o[0][1] = mfma16(Wr[2][0], H[2][1], o[0][1]);
  o[1][1] = mfma16(Wr[2][1], H[2][1], o[1][1]);
  o[2][1] = mfma16(Wr[2][2], H[2][1], o[2][1]);
  o[3][1] = mfma16(Wr[2][3], H[2][1], o[3][1]);
  o[4][1] = mfma16(Wr[2][4], H[2][1], o[4][1]);
  SCHED0();
  LD128SC1(H[2][1], vh, rws, hrd[1] + kb[10] * 4096, 0);
  asm volatile("s_waitcnt vmcnt(%1)" : "+v"(H[2][2]) : "n"(17) : "memory");
  o[0][2] = mfma16(Wr[2][0], H[2][2], o[0][2]);
  o[1][2] = mfma16(Wr[2][1], H[2][2], o[1][2]);
  o[2][2] = mfma16(Wr[2][2], H[2][2], o[2][2]);
  o[3][2] = mfma16(Wr[2][3], H[2][2], o[3][2]);
  o[4][2] = mfma16(Wr[2][4], H[2][2], o[4][2]);
  SCHED0();
  LD128SC1(H[2][2], vh, rws, hrd[2] + kb[10] * 4096, 0);
  asm volatile("s_waitcnt vmcnt(%1)" : "+v"(H[2][3]) : "n"(16) : "memory");
  o[0][3] = mfma16(Wr[2][0], H[2][3], o[0][3]);
  o[1][3] = mfma16(Wr[2][1], H[2][3], o[1][3]);
  o[2][3] = mfma16(Wr[2][2], H[2][3], o[2][3]);
  o[3][3] = mfma16(Wr[2][3], H[2][3], o[3][3]);
  o[4][3] = mfma16(Wr[2][4], H[2][3], o[4][3]);
  SCHED0();
  LD128SC1(H[2][3], vh, rws, hrd[3] + kb[10] * 4096, 0);
  asm volatile("s_waitcnt vmcnt(%1)" : "+v"(H[3][0]) : "n"(15) : "memory");
  o[0][0] = mfma16(Wr[3][0], H[3][0], o[0][0]);
  o[1][0] = mfma16(Wr[3][1], H[3][0], o[1][0]);
  o[2][0] = mfma16(Wr[3][2], H[3][0], o[2][0]);
  o[3][0] = mfma16(Wr[3][3], H[3][0], o[3][0]);
  o[4][0] = mfma16(Wr[3][4], H[3][0], o[4][0]);
  SCHED0();
  LD128SC1(H[3][0], vh, rws, hrd[0] + kb[11] * 4096, 0);
  LD128SC1(Wr[2][0], vl16, rws, w2base + kb[10] * 5120, 0);
  LD128SC1(Wr[2][1], vl16, rws, w2base + kb[10] * 5120, 1024);
  asm volatile("s_waitcnt vmcnt(%1)" : "+v"(H[3][1]) : "n"(17) : "memory");
  o[0][1] = mfma16(Wr[3][0], H[3][1], o[0][1]);
  o[1][1] = mfma16(Wr[3][1], H[3][1], o[1][1]);
  o[2][1] = mfma16(Wr[3][2], H[3][1], o[2][1]);
  o[3][1] = mfma16(Wr[3][3], H[3][1], o[3][1]);
  o[4][1] = mfma16(Wr[3][4], H[3][1], o[4][1]);
  SCHED0();
  LD128SC1(H[3][1], vh, rws, hrd[1] + kb[11] * 4096, 0);
  LD128SC1(Wr[2][2], vl16, rws, w2base + kb[10] * 5120, 2048);
  asm volatile("s_waitcnt vmcnt(%1)" : "+v"(H[3][2]) : "n"(18) : "memory");
  o[0][2] = mfma16(Wr[3][0], H[3][2], o[0][2]);
  o[1][2] = mfma16(Wr[3][1], H[3][2], o[1][2]);
  o[2][2] = mfma16(Wr[3][2], H[3][2], o[2][2]);
  o[3][2] = mfma16(Wr[3][3], H[3][2], o[3][2]);
  o[4][2] = mfma16(Wr[3][4], H[3][2], o[4][2]);
  SCHED0();
  LD128SC1(H[3][2], vh, rws, hrd[2] + kb[11] * 4096, 0);
  LD128SC1(Wr[2][3], vl16, rws, w2base + kb[10] * 5120, 3072);
  asm volatile("s_waitcnt vmcnt(%1)" : "+v"(H[3][3]) : "n"(19) : "memory");
  o[0][3] = mfma16(Wr[3][0], H[3][3], o[0][3]);
  o[1][3] = mfma16(Wr[3][1], H[3][3], o[1][3]);
  o[2][3] = mfma16(Wr[3][2], H[3][3], o[2][3]);
  o[3][3] = mfma16(Wr[3][3], H[3][3], o[3][3]);
  o[4][3] = mfma16(Wr[3][4], H[3][3], o[4][3]);
  SCHED0();
  LD128SC1(H[3][3], vh, rws, hrd[3] + kb[11] * 4096, 0);
  LD128SC1(Wr[2][4], vl16, rws, w2base + kb[10] * 5120 + 4096, 0);
  STAMPH(26);
  asm volatile("s_waitcnt vmcnt(%6)" : "+v"(H[0][0]), "+v"(Wr[0][0]), "+v"(Wr[0][1]), "+v"(Wr[0][2]), "+v"(Wr[0][3]), "+v"(Wr[0][4]) : "n"(20) : "memory");
  o[0][0] = mfma16(Wr[0][0], H[0][0], o[0][0]);
  o[1][0] = mfma16(Wr[0][1], H[0][0], o[1][0]);
  o[2][0] = mfma16(Wr[0][2], H[0][0], o[2][0]);
  o[3][0] = mfma16(Wr[0][3], H[0][0], o[3][0]);
  o[4][0] = mfma16(Wr[0][4], H[0][0], o[4][0]);
  SCHED0();
  LD128SC1(Wr[3][0], vl16, rws, w2base + kb[11] * 5120, 0);
  LD128SC1(Wr[3][1], vl16, rws, w2base + kb[11] * 5120, 1024);
  asm volatile("s_waitcnt vmcnt(%1)" : "+v"(H[0][1]) : "n"(21) : "memory");
  o[0][1] = mfma16(Wr[0][0], H[0][1], o[0][1]);
  o[1][1] = mfma16(Wr[0][1], H[0][1], o[1][1]);
  o[2][1] = mfma16(Wr[0][2], H[0][1], o[2][1]);
  o[3][1] = mfma16(Wr[0][3], H[0][1], o[3][1]);
  o[4][1] = mfma16(Wr[0][4], H[0][1], o[4][1]);
  SCHED0();
  LD128SC1(Wr[3][2], vl16, rws, w2base + kb[11] * 5120, 2048);
  asm volatile("s_waitcnt vmcnt(%1)" : "+v"(H[0][2]) : "n"(21) : "memory");
  o[0][2] = mfma16(Wr[0][0], H[0][2], o[0][2]);
  o[1][2] = mfma16(Wr[0][1], H[0][2], o[1][2]);
  o[2][2] = mfma16(Wr[0][2], H[0][2], o[2][2]);
  o[3][2] = mfma16(Wr[0][3], H[0][2], o[3][2]);
  o[4][2] = mfma16(Wr[0][4], H[0][2], o[4][2]);
  SCHED0();
  LD128SC1(Wr[3][3], vl16, rws, w2base + kb[11] * 5120, 3072);
  asm volatile("s_waitcnt vmcnt(%1)" : "+v"(H[0][3]) : "n"(21) : "memory");
  o[0][3] = mfma16(Wr[0][0], H[0][3], o[0][3]);
  o[1][3] = mfma16(Wr[0][1], H[0][3], o[1][3]);
  o[2][3] = mfma16(Wr[0][2], H[0][3], o[2][3]);
  o[3][3] = mfma16(Wr[0][3], H[0][3], o[3][3]);
  o[4][3] = mfma16(Wr[0][4], H[0][3], o[4][3]);
  SCHED0();
  LD128SC1(Wr[3][4], vl16, rws, w2base + kb[11] * 5120 + 4096, 0);
  asm volatile("s_waitcnt vmcnt(%6)" : "+v"(H[1][0]), "+v"(Wr[1][0]), "+v"(Wr[1][1]), "+v"(Wr[1][2]), "+v"(Wr[1][3]), "+v"(Wr[1][4]) : "n"(21) : "memory");
  o[0][0] = mfma16(Wr[1][0], H[1][0], o[0][0]);
  o[1][0] = mfma16(Wr[1][1], H[1][0], o[1][0]);
  o[2][0] = mfma16(Wr[1][2], H[1][0], o[2][0]);
  o[3][0] = mfma16(Wr[1][3], H[1][0], o[3][0]);
  o[4][0] = mfma16(Wr[1][4], H[1][0], o[4][0]);
  SCHED0();
  LD32SC1(sfv, vsf, rws, 0);
  asm volatile("s_waitcnt vmcnt(%1)" : "+v"(H[1][1]) : "n"(21) : "memory");
  o[0][1] = mfma16(Wr[1][0], H[1][1], o[0][1]);
  o[1][1] = mfma16(Wr[1][1], H[1][1], o[1][1]);
  o[2][1] = mfma16(Wr[1][2], H[1][1], o[2][1]);
  o[3][1] = mfma16(Wr[1][3], H[1][1], o[3][1]);
  o[4][1] = mfma16(Wr[1][4], H[1][1], o[4][1]);
  SCHED0();
  LD64(xr[0], vxo, rx, 0, 0);
  LD64(xr[1], vxo, rx, 0, 32);
  LD64(xr[2], vxo, rx, 0, 64);
  asm volatile("s_waitcnt vmcnt(%1)" : "+v"(H[1][2]) : "n"(23) : "memory");
  o[0][2] = mfma16(Wr[1][0], H[1][2], o[0][2]);
  o[1][2] = mfma16(Wr[1][1], H[1][2], o[1][2]);
  o[2][2] = mfma16(Wr[1][2], H[1][2], o[2][2]);
  o[3][2] = mfma16(Wr[1][3], H[1][2], o[3][2]);
  o[4][2] = mfma16(Wr[1][4], H[1][2], o[4][2]);
  SCHED0();
  LD64(xr[3], vxo, rx, 0, 96);
  LD64(xr[4], vxo, rx, 0, 128);
  asm volatile("s_waitcnt vmcnt(%1)" : "+v"(H[1][3]) : "n"(24) : "memory");
  o[0][3] = mfma16(Wr[1][0], H[1][3], o[0][3]);
  o[1][3] = mfma16(Wr[1][1], H[1][3], o[1][3]);
  o[2][3] = mfma16(Wr[1][2], H[1][3], o[2][3]);
  o[3][3] = mfma16(Wr[1][3], H[1][3], o[3][3]);
  o[4][3] = mfma16(Wr[1][4], H[1][3], o[4][3]);
  SCHED0();
  LD64(b2r[0], vb2, rb2, 0, 0);
  LD64(b2r[1], vb2, rb2, 0, 32);
  LD64(b2r[2], vb2, rb2, 0, 64);
  LD64(b2r[3], vb2, rb2, 0, 96);
  LD64(b2r[4], vb2, rb2, 0, 128);
  STAMPH(27);
  asm volatile("s_waitcnt vmcnt(%6)" : "+v"(H[2][0]), "+v"(Wr[2][0]), "+v"(Wr[2][1]), "+v"(Wr[2][2]), "+v"(Wr[2][3]), "+v"(Wr[2][4]) : "n"(16) : "memory");
  o[0][0] = mfma16(Wr[2][0], H[2][0], o[0][0]);
  o[1][0] = mfma16(Wr[2][1], H[2][0], o[1][0]);
  o[2][0] = mfma16(Wr[2][2], H[2][0], o[2][0]);
  o[3][0] = mfma16(Wr[2][3], H[2][0], o[3][0]);
  o[4][0] = mfma16(Wr[2][4], H[2][0], o[4][0]);
  SCHED0();
  asm volatile("s_waitcnt vmcnt(%1)" : "+v"(H[2][1]) : "n"(27) : "memory");
  o[0][1] = mfma16(Wr[2][0], H[2][1], o[0][1]);
  o[1][1] = mfma16(Wr[2][1], H[2][1], o[1][1]);
  o[2][1] = mfma16(Wr[2][2], H[2][1], o[2][1]);
  o[3][1] = mfma16(Wr[2][3], H[2][1], o[3][1]);
  o[4][1] = mfma16(Wr[2][4], H[2][1], o[4][1]);
  SCHED0();
  asm volatile("s_waitcnt vmcnt(%1)" : "+v"(H[2][2]) : "n"(26) : "memory");
  o[0][2] = mfma16(Wr[2][0], H[2][2], o[0][2]);
  o[1][2] = mfma16(Wr[2][1], H[2][2], o[1][2]);
  o[2][2] = mfma16(Wr[2][2], H[2][2], o[2][2]);
  o[3][2] = mfma16(Wr[2][3], H[2][2], o[3][2]);
  o[4][2] = mfma16(Wr[2][4], H[2][2], o[4][2]);
  SCHED0();
  asm volatile("s_waitcnt vmcnt(%1)" : "+v"(H[2][3]) : "n"(25) : "memory");
  o[0][3] = mfma16(Wr[2][0], H[2][3], o[0][3]);
  o[1][3] = mfma16(Wr[2][1], H[2][3], o[1][3]);
  o[2][3] = mfma16(Wr[2][2], H[2][3], o[2][3]);
  o[3][3] = mfma16(Wr[2][3], H[2][3], o[3][3]);
  o[4][3] = mfma16(Wr[2][4], H[2][3], o[4][3]);
  SCHED0();
  asm volatile("s_waitcnt vmcnt(%1)" : "+v"(sfv) : "n"(10) : "memory");
  STAMPR(8);
  STAMPD(23);
  if (!__all(sfv == tag)) poll_flag<SL_S>(vsf, rws, tag);
  STAMPR(9);
  LD64SC1(sr[0], vsp, rws, 0, 0);
  LD64SC1(sr[1], vsp, rws, 65536, 0);
  LD64SC1(sr[2], vsp, rws, 131072, 0);
  LD64SC1(sr[3], vsp, rws, 196608, 0);
  LD64SC1(sr[4], vsp, rws, 262144, 0);
  asm volatile("s_waitcnt vmcnt(%6)" : "+v"(H[3][0]), "+v"(Wr[3][0]), "+v"(Wr[3][1]), "+v"(Wr[3][2]), "+v"(Wr[3][3]), "+v"(Wr[3][4]) : "n"(16) : "memory");
  o[0][0] = mfma16(Wr[3][0], H[3][0], o[0][0]);
  o[1][0] = mfma16(Wr[3][1], H[3][0], o[1][0]);
  o[2][0] = mfma16(Wr[3][2], H[3][0], o[2][0]);
  o[3][0] = mfma16(Wr[3][3], H[3][0], o[3][0]);
  o[4][0] = mfma16(Wr[3][4], H[3][0], o[4][0]);
  SCHED0();
  asm volatile("s_waitcnt vmcnt(%1)" : "+v"(H[3][1]) : "n"(26) : "memory");
  o[0][1] = mfma16(Wr[3][0], H[3][1], o[0][1]);
  o[1][1] = mfma16(Wr[3][1], H[3][1], o[1][1]);
  o[2][1] = mfma16(Wr[3][2], H[3][1], o[2][1]);
  o[3][1] = mfma16(Wr[3][3], H[3][1], o[3][1]);
  o[4][1] = mfma16(Wr[3][4], H[3][1], o[4][1]);
  SCHED0();
  asm volatile("s_waitcnt vmcnt(%1)" : "+v"(H[3][2]) : "n"(24) : "memory");
  o[0][2] = mfma16(Wr[3][0], H[3][2], o[0][2]);
  o[1][2] = mfma16(Wr[3][1], H[3][2], o[1][2]);
  o[2][2] = mfma16(Wr[3][2], H[3][2], o[2][2]);
  o[3][2] = mfma16(Wr[3][3], H[3][2], o[3][2]);
  o[4][2] = mfma16(Wr[3][4], H[3][2], o[4][2]);
  SCHED0();
  asm volatile("s_waitcnt vmcnt(%1)" : "+v"(H[3][3]) : "n"(22) : "memory");
  o[0][3] = mfma16(Wr[3][0], H[3][3], o[0][3]);
  o[1][3] = mfma16(Wr[3][1], H[3][3], o[1][3]);
  o[2][3] = mfma16(Wr[3][2], H[3][3], o[2][3]);
  o[3][3] = mfma16(Wr[3][3], H[3][3], o[3][3]);
  o[4][3] = mfma16(Wr[3][4], H[3][3], o[4][3]);
  SCHED0();
#pragma unroll
  for (int ml = 1; ml < 4; ++ml)
#pragma unroll
    for (int nf = 0; nf < 5; ++nf) *(LAS f32x4*)(wl + ((ml - 1) * 5 + nf) * 1024) = o[nf][ml];
  WAITLGKM0B();
  BAR();
  f32x4 res[5];
  {
    LAS char* a1 = L + ((w - 1) & 3) * 18432 + lane * 16;
    LAS char* a2 = L + ((w - 2) & 3) * 18432 + 5120 + lane * 16;
    LAS char* a3 = L + ((w - 3) & 3) * 18432 + 10240 + lane * 16;
#pragma unroll
    for (int nf = 0; nf < 5; ++nf)
      res[nf] = (o[nf][0] + *(LAS f32x4*)(a1 + nf * 1024)) + (*(LAS f32x4*)(a2 + nf * 1024) + *(LAS f32x4*)(a3 + nf * 1024));
  }
  asm volatile("s_waitcnt vmcnt(0)"
               : "+v"(xr[0]), "+v"(xr[1]), "+v"(xr[2]), "+v"(xr[3]), "+v"(xr[4]), "+v"(b2r[0]), "+v"(b2r[1]), "+v"(b2r[2]), "+v"(b2r[3]),
                 "+v"(b2r[4]), "+v"(sr[0]), "+v"(sr[1]), "+v"(sr[2]), "+v"(sr[3]), "+v"(sr[4])::"memory");
  STAMPR(10);
#pragma unroll
  for (int nf = 0; nf < 5; ++nf) {
    u32x2 ov;
#pragma unroll
    for (int e = 0; e < 2; ++e) {
      f32x2 v = {res[nf][2 * e] + bflo(b2r[nf][e]), res[nf][2 * e + 1] + bfhi(b2r[nf][e])};
      f32x2 dd = {__builtin_amdgcn_exp2f(v.x * -1.4426950408889634f), __builtin_amdgcn_exp2f(v.y * -1.4426950408889634f)};
      dd += 1.f;
      f32x2 gg = {v.x * __builtin_amdgcn_rcpf(dd.x), v.y * __builtin_amdgcn_rcpf(dd.y)};
      f32x2 ss = {bflo(sr[nf][e]), bfhi(sr[nf][e])}, xx = {bflo(xr[nf][e]), bfhi(xr[nf][e])};
      f32x2 t = ss * gg + xx;
      ov[e] = pkbf(t.x, t.y);
    }
    __builtin_amdgcn_raw_buffer_store_b64(ov, rout, vxo + nf * 32, 0, OUT_AUX);
  }
  STAMPR(11);
}

#define PKM(d, a, b) "v_pk_mul_f32 %" #d ", %" #a ", %" #b " op_sel:[0,0] op_sel_hi:[0,1]\n\t"
#define PKF(d, a, b) "v_pk_fma_f32 %" #d ", %" #a ", %" #b ", %" #d " op_sel:[1,1,0] op_sel_hi:[1,0,1] neg_lo:[1,0,0]\n\t"
#define PKFC(d, a, b) "v_pk_fma_f32 %" #d ", %" #a ", %" #b ", %" #d " op_sel:[1,1,0] op_sel_hi:[1,0,1] neg_lo:[1,0,0] neg_hi:[1,0,1]\n\t"
__device__ __forceinline__ void cmul4(f32x2& d0, f32x2& d1, f32x2& d2, f32x2& d3, f32x2 a0, f32x2 a1, f32x2 a2, f32x2 a3, f32x2 b) {
  asm(PKM(0, 4, 8) PKM(1, 5, 8) PKM(2, 6, 8) PKM(3, 7, 8) PKF(0, 4, 8) PKF(1, 5, 8) PKF(2, 6, 8) PKF(3, 7, 8)
      : "=&v"(d0), "=&v"(d1), "=&v"(d2), "=&v"(d3) : "v"(a0), "v"(a1), "v"(a2), "v"(a3), "v"(b));
}
#define KW(p, v) *(LAS u32x4*)(p) = v
template <bool CONJ>
__device__ __forceinline__ void cms2(f32x2& d0, f32x2& d1, f32x2 a0, f32x2 b0, f32x2 a1, f32x2 b1) {
  float x0, y0, x1, y1;
  if (CONJ)
    asm("v_mul_f32 %0, %4, %6\n\tv_mul_f32 %1, %4, -%7\n\tv_mul_f32 %2, %8, %10\n\tv_mul_f32 %3, %8, %11\n\t"
        "v_fma_f32 %0, -%5, %7, %0\n\tv_fma_f32 %1, -%5, %6, %1\n\tv_fma_f32 %2, -%9, %11, %2\n\tv_fma_f32 %3, %9, %10, %3"
        : "=&v"(x0), "=&v"(y0), "=&v"(x1), "=&v"(y1) : "v"(a0.x), "v"(a0.y), "v"(b0.x), "v"(b0.y), "v"(a1.x), "v"(a1.y), "v"(b1.x), "v"(b1.y));
  else
    asm("v_mul_f32 %0, %4, %6\n\tv_mul_f32 %1, %4, %7\n\tv_mul_f32 %2, %8, %10\n\tv_mul_f32 %3, %8, %11\n\t"
        "v_fma_f32 %0, -%5, %7, %0\n\tv_fma_f32 %1, %5, %6, %1\n\tv_fma_f32 %2, -%9, %11, %2\n\tv_fma_f32 %3, %9, %10, %3"
        : "=&v"(x0), "=&v"(y0), "=&v"(x1), "=&v"(y1) : "v"(a0.x), "v"(a0.y), "v"(b0.x), "v"(b0.y), "v"(a1.x), "v"(a1.y), "v"(b1.x), "v"(b1.y));
  d0 = (f32x2){x0, y0};
  d1 = (f32x2){x1, y1};
}
template <bool HAVE, bool PK, class F>
__device__ __forceinline__ void kgen_pass(float are, float aim, float bre, float bim, float cre, float cim, float dt, LAS char* T, int lane,
                                          f32x16& kacc, u32x4 (&FA)[8], u32x4 (&FB)[8], F hook) {
#define KM(k) do { if (HAVE) { SCHED0(); kacc = mfma32(FA[k], FB[k], kacc); SCHED0(); } } while (0)
  const float a2 = dt * are * 1.4426950408889634f;
  const float br = dt * aim * 0.15915494309189535f;
  f32x2 g1, G;
  {
    const float e1 = __builtin_amdgcn_exp2f(a2), f1 = __builtin_amdgcn_fractf(br);
    g1 = (f32x2){e1 * __builtin_amdgcn_cosf(f1), e1 * __builtin_amdgcn_sinf(f1)};
    const float e2 = __builtin_amdgcn_exp2f(a2 * 256.f), f2 = __builtin_amdgcn_fractf(br * 256.f);
    G = (f32x2){e2 * __builtin_amdgcn_cosf(f2), e2 * __builtin_amdgcn_sinf(f2)};
  }
  const f32x2 Cc = {cre, cim}, Bd = {dt * bre, dt * bim};
  LAS char* tr = T + lane * 112;
  LAS char* tim = tr + 64 * 112;
  f32x2 W, g2, G2, g3, g4, G3, G4, r9, g5, g6, g7, q, r10, r13, r15, G5, G6, G7, G8, r11, q2, q4, z0, z1, z2, z3, y0, y1, y2, y3;
  if (!PK) { cms2<true>(W, g2, Cc, Bd, g1, g1); cms2<false>(G2, g4, G, G, g2, g2); cms2<false>(g3, r9, g2, g1, G, g2); cms2<false>(G3, G4, G2, G, G2, G2); }
  if (PK) asm(PKM(0, 3, 3) PKM(1, 4, 4) PKM(2, 5, 6) PKF(0, 3, 3) PKF(1, 4, 4) PKFC(2, 5, 6)
      : "=&v"(g2), "=&v"(G2), "=&v"(W) : "v"(g1), "v"(G), "v"(Cc), "v"(Bd));
  if (PK) asm(PKM(0, 5, 5) PKM(1, 5, 6) PKM(2, 7, 8) PKM(3, 7, 7) PKM(4, 8, 5) PKF(0, 5, 5) PKF(1, 5, 6) PKF(2, 7, 8) PKF(3, 7, 7) PKF(4, 8, 5)
      : "=&v"(g4), "=&v"(g3), "=&v"(G3), "=&v"(G4), "=&v"(r9) : "v"(g2), "v"(g1), "v"(G2), "v"(G));
  KM(0);
  if (!PK) { cms2<true>(q, g5, g4, g4, g4, g1); cms2<false>(g6, g7, g4, g2, g4, g3); }
  if (PK) asm(PKM(0, 4, 4) PKM(1, 4, 5) PKM(2, 4, 6) PKM(3, 4, 7) PKFC(0, 4, 4) PKF(1, 4, 5) PKF(2, 4, 6) PKF(3, 4, 7)
      : "=&v"(q), "=&v"(g5), "=&v"(g6), "=&v"(g7) : "v"(g4), "v"(g1), "v"(g2), "v"(g3));
  KW(tr, ((u32x4){pkbf(1.f, g1.x), pkbf(g2.x, g3.x), pkbf(g4.x, g5.x), pkbf(g6.x, g7.x)}));
  KW(tim, ((u32x4){pkbf(0.f, g1.y), pkbf(g2.y, g3.y), pkbf(g4.y, g5.y), pkbf(g6.y, g7.y)}));
  KM(1);
  if (!PK) { cms2<false>(q2, z1, q, q, W, q); cms2<false>(r10, r13, G, g4, G2, g4); cms2<false>(r15, r11, G3, g4, G, g6); }
  if (PK) asm(PKM(0, 6, 6) PKM(1, 7, 6) PKM(2, 8, 11) PKM(3, 9, 11) PKM(4, 10, 11) PKM(5, 8, 12)
      PKF(0, 6, 6) PKF(1, 7, 6) PKF(2, 8, 11) PKF(3, 9, 11) PKF(4, 10, 11) PKF(5, 8, 12)
      : "=&v"(q2), "=&v"(z1), "=&v"(r10), "=&v"(r13), "=&v"(r15), "=&v"(r11) : "v"(q), "v"(W), "v"(G), "v"(G2), "v"(G3), "v"(g4), "v"(g6));
  KW(tr + 16, ((u32x4){pkbf(G.x, r9.x), pkbf(r10.x, r11.x), pkbf(G2.x, r13.x), pkbf(G3.x, r15.x)}));
  KW(tim + 16, ((u32x4){pkbf(G.y, r9.y), pkbf(r10.y, r11.y), pkbf(G2.y, r13.y), pkbf(G3.y, r15.y)}));
  KM(2);
  if (!PK) { cms2<false>(q4, z2, q2, q2, W, q2); cms2<false>(z3, G5, z1, q2, G, G4); cms2<false>(G6, G7, G2, G4, G3, G4); cms2<false>(G8, y0, G4, G4, W, q4); }
  if (PK) asm(PKM(0, 3, 3) PKM(1, 4, 3) PKM(2, 5, 3) PKF(0, 3, 3) PKF(1, 4, 3) PKF(2, 5, 3)
      : "=&v"(q4), "=&v"(z2), "=&v"(z3) : "v"(q2), "v"(W), "v"(z1));
  if (PK) cmul4(G5, G6, G7, G8, G, G2, G3, G4, G4);
  KW(tr + 32, ((u32x4){pkbf(G4.x, G5.x), pkbf(G6.x, G7.x), pkbf(G8.x, 0.f), 0u}));
  KW(tim + 32, ((u32x4){pkbf(G4.y, G5.y), pkbf(G6.y, G7.y), pkbf(G8.y, 0.f), 0u}));
  KM(3);
  z0 = W;
#pragma unroll
  for (int c = 0; c < 4; ++c) {
    if (PK) cmul4(y0, y1, y2, y3, z0, z1, z2, z3, q4);
    else { cms2<false>(y0, y1, z0, q4, z1, q4); cms2<false>(y2, y3, z2, q4, z3, q4); }
    const u32x4 vr = {pkbf(z0.x, z1.x), pkbf(z2.x, z3.x), pkbf(y0.x, y1.x), pkbf(y2.x, y3.x)};
    const u32x4 vi = {pkbf(z0.y, z1.y), pkbf(z2.y, z3.y), pkbf(y0.y, y1.y), pkbf(y2.y, y3.y)};
    if (c < 3 && PK) cmul4(z0, z1, z2, z3, y0, y1, y2, y3, q4);
    if (c < 3 && !PK) { cms2<false>(z0, z1, y0, q4, y1, q4); cms2<false>(z2, z3, y2, q4, y3, q4); }
    KW(tr + 48 + 16 * c, (vr));
    KW(tim + 48 + 16 * c, (vi));
    KM(4 + c);
    if (c == 1) hook();
  }
  const int gm = lane >> 4;
  LAS char* ra = T + (8 * (gm >> 1) + ((lane >> 2) & 3)) * 112 + 32 * (gm & 1) + 8 * (lane & 3);
#pragma unroll
  for (int ks = 0; ks < 8; ++ks) {
    FA[ks] = trrd2(ra + ks * 1792, ra + ks * 1792 + 448);
    FB[ks] = trrd2(ra + ks * 1792 + 48, ra + ks * 1792 + 448 + 48);
  }
  SCHED0();
#undef KM
}

constexpr int U_STRIDE = 8336, U_ODD = 4160, KP_OFF = 57344, KP_STRIDE = 4576;

__device__ __forceinline__ void ln_role(const u16* x, const u16* w1, const u16* b1, char* ws, LAS char* L, int tg0, int strip) {
  const int tid = threadIdx.x, lane = tid & 63;
  const int w = __builtin_amdgcn_readfirstlane(tid >> 6);
  STAMPS_DECL
  STAMP(0);
  const rsrc_t rx = mkrs(x, 2048 * 640 * 2), rws = mkrs(ws, WS_SPAN);
  const int tg = tg0 + w, rr = lane >> 3, c = lane & 7;
  u32x4 xv[10];
#pragma unroll
  for (int k = 0; k < 10; ++k) xv[k] = __builtin_amdgcn_raw_buffer_load_b128(rx, (8 * tg + rr) * 1280 + k * 128 + c * 16, 0, 0);
  SCHED0();
  u32x4 cv[20];
  u32 b1w;
  const rsrc_t rw1 = mkrs(w1, strip < 0 ? 0 : 640 * 1536 * 2), rb1 = mkrs(b1, strip < 0 ? 0 : 1536 * 2);
#define CS_LOADS \
  SCHED0(); \
  _Pragma("unroll") for (int it = 0; it < 20; ++it) \
    cv[it] = __builtin_amdgcn_raw_buffer_load_b128(rw1, (160 * w + (lane >> 3)) * 3072 + strip * 128 + (lane & 7) * 16, it * 24576, 0); \
  b1w = __builtin_amdgcn_raw_buffer_load_b16(rb1, strip * 128 + lane * 2, 0, 0); \
  SCHED0();
  SCHED0();
  const u32 tag = *(cptr32)(uintptr_t)ws + TAG_ADD;
  float sa[4] = {0.f, 0.f, 0.f, 0.f};
#pragma unroll
  for (int k = 0; k < 10; ++k)
#pragma unroll
    for (int e = 0; e < 4; ++e) sa[e] = dot2bf(xv[k][e], 0x3F803F80u, sa[e]);
  STAMP(1);
  float s = (sa[0] + sa[1]) + (sa[2] + sa[3]);
  s = dpp_add<0x141>(dpp_add<0x4E>(dpp_add<0xB1>(s)));
  const float mean = s * (1.f / 640.f);
  f32x2 dv[40], qa = {0.f, 0.f}, qb = {0.f, 0.f};
#pragma unroll
  for (int k = 0; k < 40; ++k) {
    const u32 t = xv[k >> 2][k & 3];
    dv[k] = (f32x2){bflo(t), bfhi(t)} - mean;
    if (k & 1) qb += dv[k] * dv[k];
    else qa += dv[k] * dv[k];
  }
  qa += qb;
  const float q = dpp_add<0x141>(dpp_add<0x4E>(dpp_add<0xB1>(qa.x + qa.y)));
  const float var = rbf(q * (1.f / 640.f)), mb = rbf(mean);
  const float den = rbf(__builtin_amdgcn_sqrtf(rbf(var + 1e-5f)));
  const float rden = __builtin_amdgcn_rcpf(den), sh = (mean - mb) * rden;
  if (c == 0) __builtin_amdgcn_raw_buffer_store_b64((u32x2){pkbf(mb, den), tag}, rws, WS_ST + tg * ST_STR + rr * 8, 0, 16);
  LAS char* st = L + w * 10752;
#pragma unroll
  for (int k = 0; k < 10; ++k) {
    u32x4 t;
#pragma unroll
    for (int e = 0; e < 4; ++e) {
      const f32x2 n = dv[4 * k + e] * rden + sh;
      t[e] = pkbf(n.x, n.y);
    }
    *(LAS u32x4*)(st + rr * 1344 + k * 128 + c * 16) = t;
  }
  LAS char* rd = st + ((lane >> 2) & 3) * 1344 + (lane >> 4) * 32 + (lane & 3) * 8;
#pragma unroll
  for (int it = 0; it < 10; ++it) {
    const u32x4 t = trrd2(rd + it * 128, rd + it * 128 + 4 * 1344);
    __builtin_amdgcn_raw_buffer_store_b128(t, rws, WS_XN + (tg * 640 + it * 64 + lane) * 16, 0, 16);
  }
  STAMP(2);
  WAITVM(0);
  BAR();
  if (tid < 4) __builtin_amdgcn_raw_buffer_store_b32(tag, rws, WS_LNF + (tg >> 2) * LNF_STR + tid * 4, 0, 16);
  STAMP(3);
  if (strip >= 0) {
    CS_LOADS
    const int j = lane & 15;
    const u32 one = (j & 1) ? 0x3F800000u : 0x3F80u;
    const u32x4 sel = {j >> 1 == 0 ? one : 0u, j >> 1 == 1 ? one : 0u, j >> 1 == 2 ? one : 0u, j >> 1 == 3 ? one : 0u};
    f32x4 c0 = (f32x4)0.f, c1 = (f32x4)0.f;
#pragma unroll
    for (int it = 0; it < 20; it += 2) { c0 = mfma16(cv[it], sel, c0); c1 = mfma16(cv[it + 1], sel, c1); }
    c0 += c1;
    asm volatile("" : "+v"(b1w));
    LAS char* P = L + 67584;
    if (j < 8) {
#pragma unroll
      for (int e = 0; e < 4; ++e) *(LAS float*)(P + (16 * w + 4 * (lane >> 4) + e) * 32 + j * 4) = c0[e];
    }
    WAITLGKM0B();
    BAR();
    if (w == 0) {
      float cc = 0.f;
#pragma unroll
      for (int p = 0; p < 8; ++p) cc += *(LAS float*)(P + p * 256 + lane * 4);
      __builtin_amdgcn_raw_buffer_store_b128((u32x4){pkbf(cc, cc - rbf(cc)), b1w, 0u, tag}, rws, WS_CS + (64 * strip + lane) * 16, 0, 16);
    }
  }
  STAMP(6);
}

__device__ __forceinline__ void rt_role(const u16* w2, char* ws, LAS char* L, int jb0) {
  const int tid = threadIdx.x, lane = tid & 63;
  const int w = __builtin_amdgcn_readfirstlane(tid >> 6);
  STAMPS_DECL
  STAMP(0);
  const rsrc_t rws = mkrs(ws, WS_SPAN), rw2 = mkrs(w2, 1536 * 640 * 2);
  const int job = jb0 + w, cp = job >> 5, rg = job & 31;
  LAS char* T = L + w * 16896;
  u32x4 wv[15];
#pragma unroll
  for (int it = 0; it < 15; ++it) {
    const int id = it * 64 + lane, rw = id / 20, cc = id % 20;
    wv[it] = __builtin_amdgcn_raw_buffer_load_b128(rw2, (48 * rg + rw) * 1280 + cp * 320 + cc * 16, 0, 0);
  }
  const u32 tag = *(cptr32)(uintptr_t)ws + TAG_ADD;
#pragma unroll
  for (int it = 0; it < 15; ++it) {
    const int id = it * 64 + lane, rw = id / 20, cc = id % 20;
    *(LAS u32x4*)(T + rw * 352 + cc * 16) = wv[it];
  }
  STAMP(1);
  const int hl = lane >> 5;
  LAS char* rd = T + (4 * ((lane >> 4) & 1) + ((lane >> 2) & 3)) * 352 + 32 * hl + 8 * (lane & 3);
#pragma unroll
  for (int hb = 0; hb < 3; ++hb) {
    const int k16 = 3 * rg + hb;
#pragma unroll
    for (int j = 0; j < 5; ++j) {
      const u32x4 t = trrd2(rd + hb * 5632 + j * 64, rd + hb * 5632 + j * 64 + 8 * 352);
      const int nf = 2 * j + hl, tj = 2 * cp + (nf >= 5);
      __builtin_amdgcn_raw_buffer_store_b128(
          t, rws, WS_W2P + ((tj * 48 + (k16 >> 1)) * 5 + (nf >= 5 ? nf - 5 : nf)) * 1024 + (k16 & 1) * 512 + (lane & 31) * 16, 0, 16);
    }
  }
  STAMP(2);
  WAITVM(0);
  if (lane == 0) __builtin_amdgcn_raw_buffer_store_b32(tag, rws, WS_W2F + job * 128, 0, 16);
  STAMP(3);
}

__device__ __forceinline__ void epoch_role(char* ws) {
  const int lane = threadIdx.x & 63;
  if (threadIdx.x >= 64) return;
  const rsrc_t rws = mkrs(ws, WS_SPAN);
  const u32 tag = *(cptr32)(uintptr_t)ws + TAG_ADD;
  _Pragma("unroll 1") for (int sp = 0; sp < SPIN_MAX; ++sp) {
    __builtin_amdgcn_s_sleep(32);
    const u32 f = __builtin_amdgcn_raw_buffer_load_b32(rws, WS_SF + (lane & 7) * 20 * 128, 0, 16);
    if (__all(f == tag)) break;
    asm volatile("" ::: "memory");
  }
  _Pragma("unroll 1") for (int sp = 0; sp < SPIN_MAX; ++sp) {
    if (!EP_LIGHT || sp) __builtin_amdgcn_s_sleep(EP_LIGHT ? 16 : 32);
    const u32x4 lf = __builtin_amdgcn_raw_buffer_load_b128(rws, WS_LNF + lane * LNF_STR, 0, 16);
    u32 bad = (lf.x ^ tag) | (lf.y ^ tag) | (lf.z ^ tag) | (lf.w ^ tag);
#pragma unroll
    for (int u = 0; u < 2; ++u) bad |= __builtin_amdgcn_raw_buffer_load_b32(rws, WS_W2F + (u * 64 + lane) * 128, 0, 16) ^ tag;
#pragma unroll
    for (int u = 0; u < 3; ++u) bad |= __builtin_amdgcn_raw_buffer_load_b32(rws, WS_SF + (u * 48 + lane) * 128, 0, 16) ^ tag;
#pragma unroll
    for (int u = 0; u < 4; ++u) bad |= __builtin_amdgcn_raw_buffer_load_b32(rws, WS_HF + (u * 64 + lane) * 128, 0, 16) ^ tag;
    if (__all(bad == 0)) break;
    asm volatile("" ::: "memory");
  }
  if (lane == 0) __builtin_amdgcn_raw_buffer_store_b32(tag, rws, 0, 0, 16);
}

__device__ __forceinline__ void chan_role(const u16* x, const u16* w1, const u16* Are, const u16* Aim, const u16* Bre, const u16* Bim, const u16* Cre,
                                          const u16* Cim, const u16* ldt, const u16* dsk, char* ws, LAS char* L, int G) {
  const int tid = threadIdx.x, lane = tid & 63;
  const int w = __builtin_amdgcn_readfirstlane(tid >> 6);
  STAMPS_DECL
  STAMP(0);
  const rsrc_t rws = mkrs(ws, WS_SPAN);
  const int hl = lane >> 5, li = lane & 31;
  const int d = 4 * G + w;
  u32 p01[6], p2[6];
  {
    const u16* pp[6] = {Are, Aim, Bre, Bim, Cre, Cim};
#pragma unroll
    for (int k = 0; k < 6; ++k) p01[k] = *(const GAS u32*)((const GAS char*)pp[k] + d * 384 + lane * 4);
#pragma unroll
    for (int k = 0; k < 6; ++k) p2[k] = *(const GAS u16*)((const GAS char*)pp[k] + d * 384 + 256 + lane * 2);
  }
  const u32 ldw = *(cptr32)((uintptr_t)ldt + (d >> 1) * 4), dsw = *(cptr32)((uintptr_t)dsk + (d >> 1) * 4);
  const u32 tag = *(cptr32)(uintptr_t)ws + TAG_ADD;
  const float dt = rbf(expf((d & 1) ? bfhi(ldw) : bflo(ldw)));
  f32x16 kacc = (f32x16)0.f;
  LAS char* T = L + w * 14336;
  SCHED0();
  __builtin_amdgcn_s_setprio(PRIO_C);
  SCHED0();
  STAMP(1);
  STAMPC(20);
  const int ch = lane & 3, tb0 = 64 * w + 4 * (lane >> 2);
  const u32 vlnf = WS_LNF + (16 * w + (lane >> 2)) * LNF_STR + (lane & 3) * 4, vxn = WS_XN + (tb0 * 640 + 4 * G + ch) * 16;
  u32x4 uv[4];
  u32 lnf;
  bool early = false;
  {
    u32x4 FA[8], FB[8];
    kgen_pass<false, KPK1>(bflo(p01[0]), bflo(p01[1]), bflo(p01[2]), bflo(p01[3]), bflo(p01[4]), bflo(p01[5]), dt, T, lane, kacc, FA, FB, [] {});
    STAMP(2);
    kgen_pass<true, KPK2>(bfhi(p01[0]), bfhi(p01[1]), bfhi(p01[2]), bfhi(p01[3]), bfhi(p01[4]), bfhi(p01[5]), dt, T, lane, kacc, FA, FB, [&] {
    });
    __builtin_amdgcn_s_setprio(PRIO_C1);
    kgen_pass<true, KPK3>(bflo(p2[0]), bflo(p2[1]), bflo(p2[2]), bflo(p2[3]), bflo(p2[4]), bflo(p2[5]), dt, T, lane, kacc, FA, FB, [&] {
      LD32SC1(lnf, vlnf, rws, 0);
    });
#pragma unroll
    for (int ks = 0; ks < 8; ++ks) kacc = mfma32(FA[ks], FB[ks], kacc);
  }
  STAMP(3);
  STAMPC(21);
  __builtin_amdgcn_s_setprio(PRIO_C2);
  if (!early) {
    asm volatile("s_waitcnt vmcnt(0)" : "+v"(lnf)::"memory");
    if (!__all(lnf == tag))
    poll_flag<SL_L>(vlnf, rws, tag);
    LD128SC1(uv[0], vxn, rws, 0, 0); LD128SC1(uv[1], vxn, rws, 10240, 0); LD128SC1(uv[2], vxn, rws, 20480, 0); LD128SC1(uv[3], vxn, rws, 30720, 0);
  }
  STAMP(4);
  LAS char* KZ = L + KP_OFF + w * KP_STRIDE;
  LAS char* K0 = KZ + 480;
  {
    const u32x4 zero4 = {0u, 0u, 0u, 0u};
    if (lane < 30) *(LAS u32x4*)(KZ + lane * 16) = zero4;
    const float dk = (d & 1) ? bfhi(dsw) : bflo(dsw);
    const float k0 = kacc[0] + (lane == 0 ? dk : 0.f);
    *(LAS u32x2*)(K0 + li * 16 + (hl ? 0 : 8)) = (u32x2){pkbf(kacc[3], kacc[2]), pkbf(kacc[1], k0)};
    const u32 d4 = pkbf(kacc[4], kacc[4]), d5 = pkbf(kacc[5], kacc[5]), d6 = pkbf(kacc[6], kacc[6]), d7 = pkbf(kacc[7], kacc[7]);
    const u32 d8 = pkbf(kacc[8], kacc[8]), d9 = pkbf(kacc[9], kacc[9]), d10 = pkbf(kacc[10], kacc[10]), d11 = pkbf(kacc[11], kacc[11]);
    u32 n4 = shl1(d4), n6 = shl1(d6), n8 = shl1(d8), n9 = shl1(d9), n10 = shl1(d10), n11 = shl1(d11);
    const u32 f4 = __builtin_amdgcn_readlane(d6, 32), f6 = __builtin_amdgcn_readlane(d8, 0), f8 = __builtin_amdgcn_readlane(d9, 0);
    const u32 f9 = __builtin_amdgcn_readlane(d10, 0), f10 = __builtin_amdgcn_readlane(d11, 0), f11 = __builtin_amdgcn_readlane(d8, 32);
    if (lane == 63) { n4 = f4; n6 = f6; }
    if (lane == 31) { n8 = f8; n9 = f9; n10 = f10; n11 = f11; }
    const u32x4 ca = hl ? (u32x4){n4, d5, lohi(d5, d4), d4} : (u32x4){lohi(n4, d7), d6, lohi(d6, d5), d4};
    *(LAS u32x4*)(K0 + ((hl ? 64 : 32) + li) * 16) = ca;
    const bool ev2 = (li & 1) == 0;
    const u32x4 cb = hl ? (u32x4){n6, d7, lohi(d7, d6), d6} : (u32x4){n8, ev2 ? lohi(n8, d8) : n8, d8, d8};
    *(LAS u32x4*)(K0 + ((hl ? 96 : 128) + li) * 16) = cb;
    if (!hl) {
      *(LAS u32x4*)(K0 + (160 + li) * 16) = (u32x4){n9, ev2 ? lohi(n9, d9) : n9, d9, d9};
      *(LAS u32x4*)(K0 + (192 + li) * 16) = (u32x4){n10, ev2 ? lohi(n10, d10) : n10, d10, d10};
      *(LAS u32x4*)(K0 + (224 + li) * 16) = (u32x4){n11, ev2 ? lohi(n11, d11) : n11, d11, d11};
    }
  }
  WAITLGKM0B();
  BAR();
  asm volatile("s_waitcnt vmcnt(0)" : "+v"(uv[0]), "+v"(uv[1]), "+v"(uv[2]), "+v"(uv[3])::"memory");
  STAMP(9);
  {
    LAS char* E = L + ch * U_STRIDE + 32 + 16 * tb0;
    LAS char* O = E + U_ODD;
#pragma unroll
    for (int it = 0; it < 4; ++it) *(LAS u32x4*)(E + 16 * it) = uv[it];
    *(LAS u16*)(O + 2) = (u16)uv[0].x;
    *(LAS u32*)(O + 4) = __builtin_amdgcn_alignbit(uv[0].y, uv[0].x, 16);
    *(LAS u32x2*)(O + 8) = (u32x2){__builtin_amdgcn_alignbit(uv[0].z, uv[0].y, 16), __builtin_amdgcn_alignbit(uv[0].w, uv[0].z, 16)};
#pragma unroll
    for (int it = 1; it < 4; ++it)
      *(LAS u32x4*)(O + 16 * it) = (u32x4){__builtin_amdgcn_alignbit(uv[it].x, uv[it - 1].w, 16), __builtin_amdgcn_alignbit(uv[it].y, uv[it].x, 16),
                                           __builtin_amdgcn_alignbit(uv[it].z, uv[it].y, 16), __builtin_amdgcn_alignbit(uv[it].w, uv[it].z, 16)};
    *(LAS u16*)(O + 64) = (u16)(uv[3].w >> 16);
    if (tid < 4) {
      LAS char* Z = L + tid * U_STRIDE;
      const u32x4 zero4 = {0u, 0u, 0u, 0u};
      *(LAS u32x4*)(Z) = zero4; *(LAS u32x4*)(Z + 16) = zero4;
      *(LAS u32x4*)(Z + U_ODD) = zero4; *(LAS u32x4*)(Z + U_ODD + 16) = zero4;
      *(LAS u16*)(Z + U_ODD + 32) = 0;
    }
  }
  WAITLGKM0B();
  BAR();
  STAMP(5);
  STAMPC(22);
  f32x4 ya[8];
#pragma unroll
  for (int P = 0; P < 8; ++P) ya[P] = (f32x4)0.f;
  {
    const int q = lane & 15, g = lane >> 4;
    LAS char* ub[4];
#pragma unroll
    for (int k = 0; k < 4; ++k) ub[k] = L + w * U_STRIDE + ((q & 1) ? 2 * (25 + q - 8 * g) : U_ODD + 2 * (26 + q - 8 * g)) + 64 * 6 + 1024 * k;
    LAS char* kb = KZ + 32 * q + 16 * g;
    u32x4 A0[8], B0[8], A1[8], B1[8];
#define LDA(DST, K, OFF)                                                                                                     \
  {                                                                                                                          \
    const u32x2a4 lo_ = *(LAS u32x2a4*)(ub[(K) >> 1] + ((K) & 1) * 512 + (OFF)), hi_ = *(LAS u32x2a4*)(ub[(K) >> 1] + ((K) & 1) * 512 + (OFF) + 8); \
    DST = (u32x4){lo_.x, lo_.y, hi_.x, hi_.y};                                                                               \
  }
#pragma unroll
    for (int k = 0; k < 8; ++k) { B0[k] = *(LAS u32x4*)(kb + 512 * k); LDA(A0[k], k, 64) }
#pragma unroll 1
    for (int cl = 0; cl < 8; cl += 2) {
      SCHED0();
#pragma unroll
      for (int i1 = 0; i1 < 8; ++i1) {
        if (i1 < 4) {
          LDA(A1[2 * i1], 2 * i1, 0) LDA(A1[2 * i1 + 1], 2 * i1 + 1, 0)
          B1[2 * i1] = *(LAS u32x4*)(kb + 64 + 1024 * i1); B1[2 * i1 + 1] = *(LAS u32x4*)(kb + 64 + 1024 * i1 + 512);
        }
#pragma unroll
        for (int j1 = 0; j1 + i1 < 8; ++j1) ya[i1 + j1] = mfma16(A0[j1], B0[i1], ya[i1 + j1]);
        SCHED0();
      }
      kb += (cl < 6) ? 128 : 0;
#pragma unroll
      for (int k = 0; k < 4; ++k) ub[k] -= (cl < 6) ? 128 : 0;
#pragma unroll
      for (int i1 = 0; i1 < 8; ++i1) {
        if (i1 < 4) {
          LDA(A0[2 * i1], 2 * i1, 64) LDA(A0[2 * i1 + 1], 2 * i1 + 1, 64)
          B0[2 * i1] = *(LAS u32x4*)(kb + 1024 * i1); B0[2 * i1 + 1] = *(LAS u32x4*)(kb + 1024 * i1 + 512);
        }
#pragma unroll
        for (int j1 = 0; j1 + i1 < 8; ++j1) ya[i1 + j1] = mfma16(A1[j1], B1[i1], ya[i1 + j1]);
        SCHED0();
      }
    }
  }
  STAMP(6);
  STAMPC(23);
  __builtin_amdgcn_s_setprio(PRIO_C3);
  {
    LAS char* e = L + w * U_STRIDE + 32 + 32 * (lane & 15) + 8 * (lane >> 4);
#pragma unroll
    for (int P = 0; P < 8; ++P) *(LAS u32x2*)(e + 512 * P) = (u32x2){pkbf(ya[P][0], ya[P][1]), pkbf(ya[P][2], ya[P][3])};
  }
  WAITLGKM0B();
  BAR();
  {
    LAS char* rd = L + ((lane >> 2) & 3) * U_STRIDE + 32 + 2 * (512 * w + 16 * (lane >> 4) + 4 * (lane & 3));
#pragma unroll
    for (int it = 0; it < 8; ++it) {
      const u32x2 t = trrd(rd + 128 * it);
      __builtin_amdgcn_raw_buffer_store_b64(t, rws, WS_SP + G * 16384 + (512 * w + 64 * it + lane) * 8, 0, 16);
    }
  }
  STAMP(10);
  WAITVM(0);
  if (lane == 0) __builtin_amdgcn_raw_buffer_store_b32(tag, rws, WS_SF + 128 * G + SF_STRIDE * w, 0, 16);
  STAMP(7);
}

__global__ __launch_bounds__(256, 2) void fused_k(const u16* x, const u16* Are, const u16* Aim, const u16* Bre, const u16* Bim, const u16* Cre,
                                                  const u16* Cim, const u16* ldt, const u16* dsk, const u16* w1, const u16* b1, const u16* w2,
                                                  const u16* b2, u16* out, char* ws) {
  __shared__ __attribute__((aligned(16))) char smem[LDS_BYTES];
  LAS char* L = (LAS char*)smem;
  const int c = blockIdx.x & 255;
  if ((blockIdx.x >> 8) == SSM_FIRST) {
    __builtin_amdgcn_s_setprio(PRIO_G);
    gemm_role(x, w1, b2, out, ws, L, c);
  } else {
    const int xcd = c & 7, r = c >> 3;
    if (r < 20) {
      __builtin_amdgcn_s_setprio(3);
      chan_role(x, w1, Are, Aim, Bre, Bim, Cre, Cim, ldt, dsk, ws, L, 20 * xcd + r);
    } else if (r < 28) {
      __builtin_amdgcn_s_setprio(PRIO_L);
      ln_role(x, w1, b1, ws, L, 32 * xcd + 4 * (r - 20), r < 23 ? 12 * (xcd & 1) + 3 * (xcd >> 1) + r - 20 : -1);
    } else {
      __builtin_amdgcn_s_setprio(PRIO_L);
      rt_role(w2, ws, L, 16 * xcd + 4 * (r - 28));
      if (c == 224) epoch_role(ws);
    }
  }
}

extern "C" void kernel_launch(void* const* d_in, const int* in_sizes, int n_in, void* d_out, int out_size, void* d_ws, size_t ws_size,
                              hipStream_t stream) {
  fused_k<<<512, 256, 0, stream>>>((const u16*)d_in[0], (const u16*)d_in[1], (const u16*)d_in[2], (const u16*)d_in[3], (const u16*)d_in[4],
                                   (const u16*)d_in[5], (const u16*)d_in[6], (const u16*)d_in[7], (const u16*)d_in[8], (const u16*)d_in[10],
                                   (const u16*)d_in[11], (const u16*)d_in[12], (const u16*)d_in[13], (u16*)d_out, (char*)d_ws);
}
```
